```python
import torch

def _softmax(x, axis=-1):
    return torch.softmax(x, dim=axis)
def _gelu(x):
    return torch.nn.functional.gelu(x, approximate="tanh")
def _silu(x):
    return torch.nn.functional.silu(x)
def _sigmoid(x):
    return torch.sigmoid(x)
def _layernorm(x, axis=-1, eps=1e-5):
    m = x.mean(dim=axis, keepdim=True)
    v = x.var(dim=axis, keepdim=True, unbiased=False)
    return (x - m) / torch.sqrt(v + eps)
def _scatter_add(tgt, idx, src, axis=0):
    return tgt.clone().index_add_(axis, idx, src)
def _segment_sum(src, seg, n):
    out = torch.zeros((n,) + src.shape[1:], dtype=src.dtype)
    return out.index_add_(0, seg, src)
def _pad(x, pw):
    flat = tuple(v for pair in reversed(pw) for v in pair)
    return torch.nn.functional.pad(x, flat)

kernel_name = 'concat_heads__mha_block__softmax_full_mha__prompt'

def setup_inputs():

    _w = (lambda a: (lambda t: t.to(torch.bfloat16) if t.is_floating_point() else t)(torch.as_tensor(a)))
    def _A_setup():
        B, S, D, H = 1, 2048, 448, 16
        return {
            "x": _w((lambda *s: torch.randn(*s, dtype=torch.bfloat16) * 0.2)(B, S, D)),
            "wqkv": _w((lambda *s: torch.randn(*s, dtype=torch.bfloat16) * 0.2)(D, 3 * D)),
            "wo": _w((lambda *s: torch.randn(*s, dtype=torch.bfloat16) * 0.05)(D, D)),
        }
    _A_in = _A_setup()

    def _B_setup():
        B, S, D = 1, 2048, 448
        return {
            "x": _w((lambda *s: torch.randn(*s, dtype=torch.bfloat16) * 0.2)(B, S, D)),
            "wqkv": _w((lambda *s: torch.randn(*s, dtype=torch.bfloat16) * 0.2)(D, 3 * D)),
            "wo": _w((lambda *s: torch.randn(*s, dtype=torch.bfloat16) * 0.05)(D, D)),
        }
    _B_in = _B_setup()

    return {
        "A_x": _A_in["x"],
        "A_wqkv": _A_in["wqkv"],
        "A_wo": _A_in["wo"],
        "B_x": _B_in["x"],
        "B_wqkv": _B_in["wqkv"],
        "B_wo": _B_in["wo"],
    }

def reference(A_x, A_wqkv, A_wo, B_x, B_wqkv, B_wo):
    def _A_ref(x, wqkv, wo):
        B, S, D = x.shape
        H = 16
        Dh = D // H
        qkv = (lambda x, *d: x.permute(*d))((x @ wqkv).reshape(B, S, 3, H, Dh), 2, 0, 3, 1, 4)
        q, k, v = qkv[0], qkv[1], qkv[2]
        scores = torch.einsum("bhqd,bhkd->bhqk", q, k) * (Dh ** -0.5)
        p = _softmax(scores, axis=-1)
        o = torch.einsum("bhqk,bhkd->bhqd", p, v)
        return (lambda x, *d: x.permute(*d))(o, 0, 2, 1, 3).reshape(B, S, D) @ wo

    def _B_ref(x, wqkv, wo):
        B, S, D = x.shape
        H = 16; Dh = D // H
        qkv = (lambda x, *d: x.permute(*d))((x @ wqkv).reshape(B, S, 3, H, Dh), 2, 0, 3, 1, 4)
        q, k, v = qkv[0], qkv[1], qkv[2]
        scale = Dh ** -0.5
        scores = torch.einsum("bhqd,bhkd->bhqk", q, k) * scale
        p = _softmax(scores, axis=-1)
        o = torch.einsum("bhqk,bhkd->bhqd", p, v)
        return (lambda x, *d: x.permute(*d))(o, 0, 2, 1, 3).reshape(B, S, D) @ wo

    a = _A_ref(A_x, A_wqkv, A_wo)
    b = _B_ref(B_x, B_wqkv, B_wo)
    return torch.concatenate([a, b], axis=-1)
```

```cpp
#include <hip/hip_runtime.h>
#include <hip/hip_bf16.h>
#include <stdint.h>

typedef __attribute__((ext_vector_type(8))) short bf16x8;
typedef __attribute__((ext_vector_type(4))) short s16x4;
typedef __attribute__((ext_vector_type(16))) float f32x16;
typedef __attribute__((ext_vector_type(4))) unsigned u32x4;
typedef __attribute__((ext_vector_type(2))) unsigned u32x2;
typedef unsigned short u16;

#define DI __device__ __forceinline__
#define MFMA(a, b, c) __builtin_amdgcn_mfma_f32_32x32x16_bf16((a), (b), (c), 0, 0, 0)
#define LAS __attribute__((address_space(3)))

constexpr int SEQ = 2048, DM = 448, NH = 16, DH = 28, N3 = 1344;
constexpr size_t FRAG_BYTES = (size_t)2 * NH * 64 * 2048;
constexpr size_t WS_Q = 0, WS_K = FRAG_BYTES, WS_V = 2 * FRAG_BYTES, WS_O = 3 * FRAG_BYTES;
constexpr float QSCALE = 0.18898223650461362f * 1.4426950408889634f;

typedef __bf16 bf16x2_t __attribute__((ext_vector_type(2)));
typedef float f32x2_t __attribute__((ext_vector_type(2)));
DI unsigned cvtpk(float lo, float hi) { f32x2_t v = {lo, hi}; return __builtin_bit_cast(unsigned, __builtin_convertvector(v, bf16x2_t)); }

DI void glds16(const void* sbase, unsigned voff, unsigned lds_base) {
    unsigned sv;
    asm volatile("s_mov_b32 %0, m0\n\ts_mov_b32 m0, %3\n\ts_nop 0\n\tglobal_load_lds_dwordx4 %1, %2\n\ts_mov_b32 m0, %0"
                 : "=&s"(sv) : "v"(voff), "s"(sbase), "s"(lds_base) : "memory");
}
typedef short v4i16_t __attribute__((ext_vector_type(4)));
DI s16x4 trread(const LAS char* p) { return __builtin_bit_cast(s16x4, __builtin_amdgcn_ds_read_tr16_b64_v4i16((LAS v4i16_t*)p)); }
DI bf16x8 ldsread16(const LAS char* p) { return *(const LAS bf16x8*)p; }
DI bf16x8 cat8(s16x4 lo, s16x4 hi) { return __builtin_shufflevector(lo, hi, 0, 1, 2, 3, 4, 5, 6, 7); }

constexpr int Q_SLOT = 40960, Q_NSLOT = 3, Q_WOFF = 16384, Q_WROW = 336;

template <int HT>
DI void qkv_compute_stage(const LAS char* sb, int xbase0, int wbase, f32x16 (&acc)[6]) {
#pragma unroll
    for (int ks = 0; ks < 4; ++ks) {
        bf16x8 xa0 = ldsread16(sb + (xbase0 ^ (ks << 5)));
        bf16x8 xa1 = ldsread16(sb + ((xbase0 ^ (ks << 5)) + 4096));
#pragma unroll
        for (int i = 0; i < 3; ++i) {
            const int slot = HT * 3 + i, sec = slot >> 1, e = slot & 1;
            const LAS char* wp = sb + wbase + ks * 16 * Q_WROW + sec * 112 + e * 56;
            bf16x8 wf = cat8(trread(wp), trread(wp + 4 * Q_WROW));
            if (sec < 2) { acc[i] = MFMA(wf, xa0, acc[i]); acc[3 + i] = MFMA(wf, xa1, acc[3 + i]); }
            else         { acc[i] = MFMA(xa0, wf, acc[i]); acc[3 + i] = MFMA(xa1, wf, acc[3 + i]); }
        }
    }
}

template <int HT>
DI void qkv_epilogue(f32x16 (&acc)[6], char* ws, int blk, int hp, int tile0, int lane) {
    const int h = lane >> 5, r = lane & 31;
#pragma unroll
    for (int st = 0; st < 2; ++st) {
#pragma unroll
        for (int i = 0; i < 3; ++i) {
            const int slot = HT * 3 + i, sec = slot >> 1, e = slot & 1;
            f32x16 a = acc[st * 3 + i];
            const int bh = blk * 16 + hp * 2 + e;
            char* dst = ws + (size_t)sec * FRAG_BYTES + ((size_t)(bh * 64 + tile0 + st) * 2) * 1024 + lane * 16;
            u32x4 p0, p1;
            if (sec == 0) {
#pragma unroll
                for (int j = 0; j < 16; ++j) a[j] *= QSCALE;
            }
            p0 = (u32x4){cvtpk(a[0], a[1]), cvtpk(a[2], a[3]), cvtpk(a[4], a[5]), cvtpk(a[6], a[7])};
            p1 = (u32x4){cvtpk(a[8], a[9]), cvtpk(a[10], a[11]), cvtpk(a[12], a[13]), cvtpk(a[14], a[15])};
            if (sec < 2) {
                if (h) { p1[2] = 0u; p1[3] = 0u; }
            } else {
                if (r >= 28) { const unsigned f = (r == 28) ? 0x3F803F80u : 0u; p0 = (u32x4){f, f, f, f}; p1 = p0; }
            }
            *(u32x4*)dst = p0;
            *(u32x4*)(dst + 1024) = p1;
        }
    }
}

__global__ __launch_bounds__(256, 1) void qkv_kernel(const u16* __restrict__ xA, const u16* __restrict__ wA,
                                                     const u16* __restrict__ xB, const u16* __restrict__ wB, char* __restrict__ ws) {
    __shared__ __attribute__((aligned(16))) char lds_[Q_SLOT * Q_NSLOT];
    const LAS char* lds = (const LAS char*)lds_;
    const unsigned lds0 = (unsigned)(uintptr_t)lds_;
    const int tid = threadIdx.x, lane = tid & 63;
    const int w = __builtin_amdgcn_readfirstlane(tid >> 6);
    const int bid = blockIdx.x, g = bid & 7, rr = bid >> 3;
    const int blk = g >> 2, hp = (g & 3) * 2 + (rr & 1), tt = rr >> 1;
    const u16* x = blk ? xB : xA;
    const u16* wq = blk ? wB : wA;
    const char* xs = (const char*)(x + (size_t)tt * 128 * DM);
    const char* wsrc = (const char*)(wq + hp * 56);

    unsigned xoff;
    { const int row = 8 * w + (lane >> 3), slot = lane & 7, c = slot ^ ((row >> 1) & 7); xoff = (unsigned)(row * DM * 2 + c * 16); }
    unsigned woff[6];
#pragma unroll
    for (int j = 0; j < 6; ++j) {
        int ci = (j * 4 + w) * 64 + lane; if (ci >= 1344) ci -= 1344;
        const int row = ci / 21, ch = ci - row * 21, sec = ch / 7, cc = ch - sec * 7;
        woff[j] = (unsigned)(row * N3 * 2 + sec * DM * 2 + cc * 16);
    }
    auto issue = [&](int s) {
        const unsigned sl = lds0 + (unsigned)((s % Q_NSLOT) * Q_SLOT);
        const char* xb = xs + s * 128;
        const char* wb = wsrc + (size_t)s * 64 * N3 * 2;
#pragma unroll
        for (int j = 0; j < 4; ++j) glds16(xb + (size_t)j * 32 * DM * 2, xoff, sl + (j * 4 + w) * 1024);
#pragma unroll
        for (int j = 0; j < 6; ++j) glds16(wb, woff[j], sl + Q_WOFF + (j * 4 + w) * 1024);
    };
    issue(0); issue(1);

    const int h = lane >> 5, r = lane & 31;
    const int tp = w & 1, ht = w >> 1;
    const int xbase0 = (64 * tp + r) * 128 + ((h ^ ((r >> 1) & 7)) << 4);
    const int i16 = lane & 15, gq = (lane >> 4) & 1;
    const int wbase = Q_WOFF + (8 * h + (i16 >> 2)) * Q_WROW + (16 * gq + 4 * (i16 & 3)) * 2;

    f32x16 acc[6];
#pragma unroll
    for (int i = 0; i < 6; ++i) acc[i] = (f32x16){};

    for (int s = 0; s < 7; ++s) {
        if (s < 6) asm volatile("s_waitcnt vmcnt(10) lgkmcnt(0)\n\ts_barrier" ::: "memory");
        else       asm volatile("s_waitcnt vmcnt(0) lgkmcnt(0)\n\ts_barrier" ::: "memory");
        if (s + 2 < 7) issue(s + 2);
        const LAS char* sb = lds + (s % Q_NSLOT) * Q_SLOT;
        if (ht == 0) qkv_compute_stage<0>(sb, xbase0, wbase, acc);
        else         qkv_compute_stage<1>(sb, xbase0, wbase, acc);
    }
    if (ht == 0) qkv_epilogue<0>(acc, ws, blk, hp, tt * 4 + tp * 2, lane);
    else         qkv_epilogue<1>(acc, ws, blk, hp, tt * 4 + tp * 2, lane);
}

constexpr int A_CHUNK = 16384, A_NSLOT = 4;

DI float rowmax16(const f32x16& s) {
    float a = fmaxf(fmaxf(s[0], s[1]), s[2]);
#pragma unroll
    for (int i = 3; i < 15; i += 2) a = fmaxf(fmaxf(a, s[i]), s[i + 1]);
    a = fmaxf(a, s[15]);
    auto rr = __builtin_amdgcn_permlane32_swap(__float_as_uint(a), __float_as_uint(a), false, false);
    return fmaxf(__uint_as_float(rr[0]), __uint_as_float(rr[1]));
}
DI void pack16(const f32x16& p, bf16x8& p0, bf16x8& p1) {
    u32x4 a = {cvtpk(p[0], p[1]), cvtpk(p[2], p[3]), cvtpk(p[4], p[5]), cvtpk(p[6], p[7])};
    u32x4 b = {cvtpk(p[8], p[9]), cvtpk(p[10], p[11]), cvtpk(p[12], p[13]), cvtpk(p[14], p[15])};
    p0 = __builtin_bit_cast(bf16x8, a); p1 = __builtin_bit_cast(bf16x8, b);
}

DI void attn_slow(const char* kg, const char* vg, bf16x8 q0, bf16x8 q1, f32x16& o) {
    float m = -INFINITY;
    o = (f32x16){};
    for (int t = 0; t < 64; ++t) {
        bf16x8 k0 = *(const bf16x8*)(kg + t * 2048), k1 = *(const bf16x8*)(kg + t * 2048 + 1024);
        bf16x8 v0 = *(const bf16x8*)(vg + t * 2048), v1 = *(const bf16x8*)(vg + t * 2048 + 1024);
        f32x16 s = MFMA(k0, q0, (f32x16){}); s = MFMA(k1, q1, s);
        const float mn = fmaxf(m, rowmax16(s));
        const float alpha = (m == -INFINITY) ? 0.f : __builtin_amdgcn_exp2f(m - mn);
        m = mn;
#pragma unroll
        for (int i = 0; i < 16; ++i) { o[i] *= alpha; s[i] = __builtin_amdgcn_exp2f(s[i] - mn); }
        bf16x8 p0, p1; pack16(s, p0, p1);
        o = MFMA(v0, p0, o); o = MFMA(v1, p1, o);
    }
}

__global__ __launch_bounds__(512, 2) void attn_kernel(char* __restrict__ ws) {
    __shared__ __attribute__((aligned(16))) char lds_[A_CHUNK * A_NSLOT];
    const LAS char* lds = (const LAS char*)lds_;
    const unsigned lds0 = (unsigned)(uintptr_t)lds_;
    const int tid = threadIdx.x, lane = tid & 63;
    const int w = __builtin_amdgcn_readfirstlane(tid >> 6);
    const int bid = blockIdx.x;
    const int bh = (bid & 7) * 4 + ((bid >> 3) & 3), qb = bid >> 5;
    const char* kbase = ws + WS_K + (size_t)bh * 131072;
    const char* vbase = ws + WS_V + (size_t)bh * 131072;
    const unsigned loff = (unsigned)(w * 1024 + lane * 16);
    auto issue = [&](int c) {
        const unsigned sl = lds0 + (unsigned)((c & (A_NSLOT - 1)) * A_CHUNK) + w * 1024;
        glds16(kbase + c * 8192, loff, sl);
        glds16(vbase + c * 8192, loff, sl + 8192);
    };
    const char* qp = ws + WS_Q + (size_t)bh * 131072 + (size_t)(qb * 8 + w) * 2048 + lane * 16;
    bf16x8 q0 = *(const bf16x8*)qp, q1 = *(const bf16x8*)(qp + 1024);
    issue(0); issue(1); issue(2);

    f32x16 o = (f32x16){};
    f32x16 negm;
    const LAS char* lp = lds + lane * 16;
    for (int c = 0; c < 16; ++c) {
        if (c < 14)       asm volatile("s_waitcnt vmcnt(4) lgkmcnt(0)\n\ts_barrier" ::: "memory");
        else if (c == 14) asm volatile("s_waitcnt vmcnt(2) lgkmcnt(0)\n\ts_barrier" ::: "memory");
        else              asm volatile("s_waitcnt vmcnt(0) lgkmcnt(0)\n\ts_barrier" ::: "memory");
        if (c + 3 < 16) issue(c + 3);
        const LAS char* cb = lp + (c & (A_NSLOT - 1)) * A_CHUNK;
        if (c == 0) {
            f32x16 s = MFMA(ldsread16(cb), q0, (f32x16){}); s = MFMA(ldsread16(cb + 1024), q1, s);
            const float nm = -rowmax16(s);
#pragma unroll
            for (int i = 0; i < 16; ++i) negm[i] = nm;
        }
#pragma unroll
        for (int t = 0; t < 4; ++t) {
            bf16x8 k0 = ldsread16(cb + t * 2048), k1 = ldsread16(cb + t * 2048 + 1024);
            bf16x8 v0 = ldsread16(cb + 8192 + t * 2048), v1 = ldsread16(cb + 8192 + t * 2048 + 1024);
            f32x16 s = MFMA(k0, q0, negm); s = MFMA(k1, q1, s);
#pragma unroll
            for (int i = 0; i < 16; ++i) s[i] = __builtin_amdgcn_exp2f(s[i]);
            bf16x8 p0, p1; pack16(s, p0, p1);
            o = MFMA(v0, p0, o); o = MFMA(v1, p1, o);
        }
    }
    {
        float amax = 0.f; bool bad = false;
#pragma unroll
        for (int i = 0; i < 16; ++i) { const float a = fabsf(o[i]); bad |= !(a < 1.0e30f); amax = fmaxf(amax, a); }
        if (__any(bad)) attn_slow(kbase + lane * 16, vbase + lane * 16, q0, q1, o);
    }
    const int h = lane >> 5, r = lane & 31;
    const float l = __shfl(o[12], r + 32);
    const float inv = 1.0f / l;
    const int blk = bh >> 4, head = bh & 15;
    char* op = ws + WS_O + ((size_t)(blk * SEQ + qb * 256 + w * 32 + r) * DM + head * DH + 4 * h) * 2;
#pragma unroll
    for (int gg = 0; gg < 4; ++gg) {
        u32x2 v = {cvtpk(o[4 * gg] * inv, o[4 * gg + 1] * inv), cvtpk(o[4 * gg + 2] * inv, o[4 * gg + 3] * inv)};
        if (gg < 3 || h == 0) *(u32x2*)(op + gg * 16) = v;
    }
}

__global__ __launch_bounds__(512, 2) void oproj_kernel(const u16* __restrict__ woA, const u16* __restrict__ woB,
                                                       const char* __restrict__ ws, u16* __restrict__ out) {
    __shared__ __attribute__((aligned(16))) char lds_[448 * 128];
    const LAS char* lds = (const LAS char*)lds_;
    const unsigned lds0 = (unsigned)(uintptr_t)lds_;
    const int tid = threadIdx.x, lane = tid & 63;
    const int w = __builtin_amdgcn_readfirstlane(tid >> 6);
    const int bid = blockIdx.x;
    const int ng = bid % 7, tg = (bid / 7) & 15, blk = bid / 112;
    const u16* wo = blk ? woB : woA;
    const char* wsrc = (const char*)(wo + ng * 64);
#pragma unroll
    for (int j = 0; j < 7; ++j) {
        const int ci = (j * 8 + w) * 64 + lane, k = ci >> 3, slot = ci & 7, ch = slot ^ (((k >> 1) & 1) << 2);
        glds16(wsrc, (unsigned)(k * DM * 2 + ch * 16), lds0 + (j * 8 + w) * 1024);
    }
    const int h = lane >> 5, r = lane & 31;
    const int st = w & 3, nt = w >> 2;
    const int tok = tg * 128 + st * 32 + r;
    const char* ap = ws + WS_O + ((size_t)(blk * SEQ + tok) * DM) * 2 + h * 16;
    bf16x8 of[28];
#pragma unroll
    for (int ks = 0; ks < 28; ++ks) of[ks] = *(const bf16x8*)(ap + ks * 32);
    asm volatile("s_waitcnt vmcnt(0) lgkmcnt(0)\n\ts_barrier" ::: "memory");
    const int i16 = lane & 15, gq = (lane >> 4) & 1, q = i16 >> 2;
    const LAS char* wp = lds + (8 * h + q) * 128 + ((nt ^ ((q >> 1) & 1)) * 64) + (16 * gq + 4 * (i16 & 3)) * 2;
    f32x16 acc = (f32x16){};
#pragma unroll
    for (int ks = 0; ks < 28; ++ks) {
        bf16x8 wf = cat8(trread(wp + ks * 2048), trread(wp + ks * 2048 + 512));
        acc = MFMA(wf, of[ks], acc);
    }
    u32x2 pk[4];
#pragma unroll
    for (int gg = 0; gg < 4; ++gg) pk[gg] = (u32x2){cvtpk(acc[4 * gg], acc[4 * gg + 1]), cvtpk(acc[4 * gg + 2], acc[4 * gg + 3])};
    char* dst = (char*)(out + (size_t)tok * (2 * DM) + blk * DM + ng * 64 + nt * 32 + 8 * h);
#pragma unroll
    for (int gg = 0; gg < 4; gg += 2) {
        auto r0 = __builtin_amdgcn_permlane32_swap(pk[gg][0], pk[gg + 1][0], false, false);
        auto r1 = __builtin_amdgcn_permlane32_swap(pk[gg][1], pk[gg + 1][1], false, false);
        u32x4 v = {r0[0], r1[0], r0[1], r1[1]};
        *(u32x4*)(dst + gg * 16) = v;
    }
}

extern "C" void kernel_launch(void* const* d_in, const int* in_sizes, int n_in,
                              void* d_out, int out_size, void* d_ws, size_t ws_size,
                              hipStream_t stream) {
    const u16* xA = (const u16*)d_in[0]; const u16* wA = (const u16*)d_in[1]; const u16* woA = (const u16*)d_in[2];
    const u16* xB = (const u16*)d_in[3]; const u16* wB = (const u16*)d_in[4]; const u16* woB = (const u16*)d_in[5];
    char* ws = (char*)d_ws;
    qkv_kernel<<<256, 256, 0, stream>>>(xA, wA, xB, wB, ws);
    attn_kernel<<<256, 512, 0, stream>>>(ws);
    oproj_kernel<<<224, 512, 0, stream>>>(woA, woB, ws, (u16*)d_out);
}
```

```cpp
#include <hip/hip_runtime.h>
#include <hip/hip_bf16.h>
#include <stdint.h>

typedef __attribute__((ext_vector_type(8))) short bf16x8;
typedef __attribute__((ext_vector_type(4))) short s16x4;
typedef __attribute__((ext_vector_type(16))) float f32x16;
typedef __attribute__((ext_vector_type(4))) unsigned u32x4;
typedef __attribute__((ext_vector_type(2))) unsigned u32x2;
typedef unsigned short u16;

#define DI __device__ __forceinline__
#define MFMA(a, b, c) __builtin_amdgcn_mfma_f32_32x32x16_bf16((a), (b), (c), 0, 0, 0)
#define LAS __attribute__((address_space(3)))
#define DIAG_STAMP(wsp, kidx, i)
#define DIAG_BEGIN()
#define DIAG_END(wsp, kidx)

#define SB() __builtin_amdgcn_sched_barrier(0)
#define PIN(x) asm volatile("" : "+v"(x))
constexpr int SEQ = 2048, DM = 448, NH = 16, DH = 28, N3 = 1344;
constexpr size_t FRAG_BYTES = (size_t)2 * NH * 64 * 2048;
constexpr size_t WS_Q = 0, WS_K = FRAG_BYTES, WS_V = 2 * FRAG_BYTES, WS_O = 3 * FRAG_BYTES;
constexpr float QSCALE = 0.18898223650461362f * 1.4426950408889634f;

typedef __bf16 bf16x2_t __attribute__((ext_vector_type(2)));
typedef float f32x2_t __attribute__((ext_vector_type(2)));
DI unsigned cvtpk(float lo, float hi) { f32x2_t v = {lo, hi}; return __builtin_bit_cast(unsigned, __builtin_convertvector(v, bf16x2_t)); }

DI void glds16(const void* sbase, unsigned voff, unsigned lds_base) {
    unsigned sv;
    asm volatile("s_mov_b32 %0, m0\n\ts_mov_b32 m0, %3\n\ts_nop 0\n\tglobal_load_lds_dwordx4 %1, %2\n\ts_mov_b32 m0, %0"
                 : "=&s"(sv) : "v"(voff), "s"(sbase), "s"(lds_base) : "memory");
}
typedef short v4i16_t __attribute__((ext_vector_type(4)));
DI s16x4 trread(const LAS char* p) { return __builtin_bit_cast(s16x4, __builtin_amdgcn_ds_read_tr16_b64_v4i16((LAS v4i16_t*)p)); }
DI bf16x8 ldsread16(const LAS char* p) { return *(const LAS bf16x8*)p; }
DI bf16x8 cat8(s16x4 lo, s16x4 hi) { return __builtin_shufflevector(lo, hi, 0, 1, 2, 3, 4, 5, 6, 7); }

constexpr int Q_SLOT = 40960, Q_NSLOT = 4, Q_WOFF = 16384, Q_WROW = 336;

template <int NQ>
DI void qkv_compute(const LAS char* lds, int xbase0, int wbase, char* ws, int blk, int hs0, int tile0, int lane
) {
    f32x16 acc[6];
#pragma unroll
    for (int i = 0; i < 6; ++i) acc[i] = (f32x16){};
    bf16x8 xa[2][2], wf[2][3];
    auto ldfr = [&](int kk, int buf) {
        const LAS char* sb = lds + ((kk >> 2) % Q_NSLOT) * Q_SLOT; const int ks = kk & 3;
        xa[buf][0] = ldsread16(sb + (xbase0 ^ (ks << 5)));
        xa[buf][1] = ldsread16(sb + ((xbase0 ^ (ks << 5)) + 4096));
#pragma unroll
        for (int i = 0; i < 3; ++i) {
            const LAS char* wp = sb + wbase + ks * 16 * Q_WROW + i * 56;
            wf[buf][i] = cat8(trread(wp), trread(wp + 4 * Q_WROW));
        }
    };
    asm volatile("s_waitcnt lgkmcnt(0)\n\ts_barrier" ::: "memory");
    DIAG_STAMP(ws, 0, 0);
    ldfr(0, 0);
#pragma unroll
    for (int kk = 0; kk < 28; ++kk) {
        const int cur = kk & 1;
        if (kk + 1 < 28) {
            if (((kk + 1) & 3) == 0) { asm volatile("s_waitcnt lgkmcnt(0)\n\ts_barrier" ::: "memory"); DIAG_STAMP(ws, 0, (kk + 1) >> 2); }
            ldfr(kk + 1, cur ^ 1);
        }
        SB();
#pragma unroll
        for (int i = 0; i < 3; ++i) {
            if (i < NQ) { acc[i] = MFMA(wf[cur][i], xa[cur][0], acc[i]); acc[3 + i] = MFMA(wf[cur][i], xa[cur][1], acc[3 + i]); }
            else        { acc[i] = MFMA(xa[cur][0], wf[cur][i], acc[i]); acc[3 + i] = MFMA(xa[cur][1], wf[cur][i], acc[3 + i]); }
        }
        SB();
    }
    DIAG_STAMP(ws, 0, 7);
    const int h = lane >> 5, r = lane & 31;
#pragma unroll
    for (int i = 0; i < 3; ++i) {
        const int hs = hs0 + i, sec = hs >> 4, bh = blk * 16 + (hs & 15);
        const float sc = (sec == 0) ? QSCALE : 1.0f;
#pragma unroll
        for (int st = 0; st < 2; ++st) {
            f32x16 a = acc[st * 3 + i];
            char* dst = ws + (size_t)sec * FRAG_BYTES + ((size_t)(bh * 64 + tile0 + st) * 2) * 1024 + lane * 16;
            if (i < NQ) {
#pragma unroll
                for (int j = 0; j < 16; ++j) a[j] *= sc;
            }
            u32x4 p0 = {cvtpk(a[0], a[1]), cvtpk(a[2], a[3]), cvtpk(a[4], a[5]), cvtpk(a[6], a[7])};
            u32x4 p1 = {cvtpk(a[8], a[9]), cvtpk(a[10], a[11]), cvtpk(a[12], a[13]), cvtpk(a[14], a[15])};
            if (i < NQ) {
                if (h) { p1[2] = 0u; p1[3] = 0u; }
            } else {
                if (r >= 28) { const unsigned f = (r == 28) ? 0x3F803F80u : 0u; p0 = (u32x4){f, f, f, f}; p1 = p0; }
            }
            asm volatile("global_store_dwordx4 %0, %1, off sc1\n\ts_nop 1" :: "v"(dst), "v"(p0) : "memory");
            asm volatile("global_store_dwordx4 %0, %1, off offset:1024 sc1\n\ts_nop 1" :: "v"(dst), "v"(p1) : "memory");
        }
    }
}

__global__ __launch_bounds__(512, 2) void qkv_kernel(const u16* __restrict__ xA, const u16* __restrict__ wA,
                                                     const u16* __restrict__ xB, const u16* __restrict__ wB, char* __restrict__ ws) {
    DIAG_BEGIN();
    __shared__ __attribute__((aligned(16))) char lds_[Q_SLOT * Q_NSLOT];
    const LAS char* lds = (const LAS char*)lds_;
    const unsigned lds0 = (unsigned)(uintptr_t)lds_;
    const int tid = threadIdx.x, lane = tid & 63;
    const int wv = __builtin_amdgcn_readfirstlane(tid >> 6), w = wv & 3;
    const int bid = blockIdx.x, g = bid & 7, rr = bid >> 3;
    const int blk = g >> 2, tt = (g & 1) * 8 + (rr & 7), cg = ((g >> 1) & 1) * 4 + (rr >> 3);
    if (wv >= 4) {
        const u16* x = blk ? xB : xA;
        const u16* wq = blk ? wB : wA;
        const char* xs = (const char*)(x + (size_t)tt * 128 * DM);
        const char* wsrc = (const char*)(wq + cg * 168);
        unsigned xoff;
        { const int row = 8 * w + (lane >> 3), slot = lane & 7, c = slot ^ ((row >> 1) & 7); xoff = (unsigned)(row * DM * 2 + c * 16); }
        unsigned woff[6];
#pragma unroll
        for (int j = 0; j < 6; ++j) {
            int ci = (j * 4 + w) * 64 + lane; if (ci >= 1344) ci -= 1344;
            const int row = ci / 21, ch = ci - row * 21;
            woff[j] = (unsigned)(row * N3 * 2 + ch * 16);
        }
        auto issue = [&](int s) {
            const unsigned sl = lds0 + (unsigned)((s % Q_NSLOT) * Q_SLOT);
            const char* xb = xs + s * 128;
            const char* wb = wsrc + (size_t)s * 64 * N3 * 2;
#pragma unroll
            for (int j = 0; j < 4; ++j) glds16(xb + (size_t)j * 32 * DM * 2, xoff, sl + (j * 4 + w) * 1024);
#pragma unroll
            for (int j = 0; j < 6; ++j) glds16(wb, woff[j], sl + Q_WOFF + (j * 4 + w) * 1024);
        };
        issue(0); issue(1); issue(2);
#pragma unroll
        for (int s = 0; s < 7; ++s) {
            if (s < 5)       asm volatile("s_waitcnt vmcnt(20)\n\ts_barrier" ::: "memory");
            else if (s == 5) asm volatile("s_waitcnt vmcnt(10)\n\ts_barrier" ::: "memory");
            else             asm volatile("s_waitcnt vmcnt(0)\n\ts_barrier" ::: "memory");
            if (s + 3 < 7) issue(s + 3);
        }
        return;
    }
    const int h = lane >> 5, r = lane & 31;
    const int tp = w & 1, ht = w >> 1;
    const int xbase0 = (64 * tp + r) * 128 + ((h ^ ((r >> 1) & 7)) << 4);
    const int i16 = lane & 15, gq = (lane >> 4) & 1;
    const int wbase = Q_WOFF + (8 * h + (i16 >> 2)) * Q_WROW + (16 * gq + 4 * (i16 & 3)) * 2 + ht * 168;
    const int hs0 = cg * 6 + ht * 3;
    const int nq = hs0 >= 32 ? 0 : (32 - hs0 >= 3 ? 3 : 32 - hs0);
    const int tile0 = tt * 4 + tp * 2;
#define DG
    if (nq == 3)      qkv_compute<3>(lds, xbase0, wbase, ws, blk, hs0, tile0, lane DG);
    else if (nq == 0) qkv_compute<0>(lds, xbase0, wbase, ws, blk, hs0, tile0, lane DG);
    else              qkv_compute<2>(lds, xbase0, wbase, ws, blk, hs0, tile0, lane DG);
    DIAG_END(ws, 0);
}

constexpr int A_CHUNK = 16384, A_NSLOT = 4;

#define EXP4(v, i) do { v[i] = __builtin_amdgcn_exp2f(v[i]); v[i + 1] = __builtin_amdgcn_exp2f(v[i + 1]); v[i + 2] = __builtin_amdgcn_exp2f(v[i + 2]); v[i + 3] = __builtin_amdgcn_exp2f(v[i + 3]); } while (0)
DI float rowmax16(const f32x16& s) {
    float a = fmaxf(fmaxf(s[0], s[1]), s[2]);
#pragma unroll
    for (int i = 3; i < 15; i += 2) a = fmaxf(fmaxf(a, s[i]), s[i + 1]);
    a = fmaxf(a, s[15]);
    auto rr = __builtin_amdgcn_permlane32_swap(__float_as_uint(a), __float_as_uint(a), false, false);
    return fmaxf(__uint_as_float(rr[0]), __uint_as_float(rr[1]));
}
DI void pack16(const f32x16& p, bf16x8& p0, bf16x8& p1) {
    u32x4 a = {cvtpk(p[0], p[1]), cvtpk(p[2], p[3]), cvtpk(p[4], p[5]), cvtpk(p[6], p[7])};
    u32x4 b = {cvtpk(p[8], p[9]), cvtpk(p[10], p[11]), cvtpk(p[12], p[13]), cvtpk(p[14], p[15])};
    p0 = __builtin_bit_cast(bf16x8, a); p1 = __builtin_bit_cast(bf16x8, b);
}

DI void attn_slow(const char* kg, const char* vg, bf16x8 q0, bf16x8 q1, f32x16& o) {
    float m = -INFINITY;
    o = (f32x16){};
    for (int t = 0; t < 64; ++t) {
        bf16x8 k0 = *(const bf16x8*)(kg + t * 2048), k1 = *(const bf16x8*)(kg + t * 2048 + 1024);
        bf16x8 v0 = *(const bf16x8*)(vg + t * 2048), v1 = *(const bf16x8*)(vg + t * 2048 + 1024);
        f32x16 s = MFMA(k0, q0, (f32x16){}); s = MFMA(k1, q1, s);
        const float mn = fmaxf(m, rowmax16(s));
        const float alpha = (m == -INFINITY) ? 0.f : __builtin_amdgcn_exp2f(m - mn);
        m = mn;
#pragma unroll
        for (int i = 0; i < 16; ++i) { o[i] *= alpha; s[i] = __builtin_amdgcn_exp2f(s[i] - mn); }
        bf16x8 p0, p1; pack16(s, p0, p1);
        o = MFMA(v0, p0, o); o = MFMA(v1, p1, o);
    }
}

__global__ __launch_bounds__(512, 2) void attn_kernel(char* __restrict__ ws) {
    DIAG_BEGIN();
    __shared__ __attribute__((aligned(16))) char lds_[A_CHUNK * A_NSLOT];
    const LAS char* lds = (const LAS char*)lds_;
    const unsigned lds0 = (unsigned)(uintptr_t)lds_;
    const int tid = threadIdx.x, lane = tid & 63;
    const int w = __builtin_amdgcn_readfirstlane(tid >> 6);
    const int bid = blockIdx.x;
    const int bh = (bid & 7) * 4 + ((bid >> 3) & 3), qb = bid >> 5;
    const char* kbase = ws + WS_K + (size_t)bh * 131072;
    const char* vbase = ws + WS_V + (size_t)bh * 131072;
    const unsigned loff = (unsigned)(w * 1024 + lane * 16);
    auto issue = [&](int c) {
        const unsigned sl = lds0 + (unsigned)((c & (A_NSLOT - 1)) * A_CHUNK) + w * 1024;
        glds16(kbase + c * 8192, loff, sl);
        glds16(vbase + c * 8192, loff, sl + 8192);
    };
    const char* qp = ws + WS_Q + (size_t)bh * 131072 + (size_t)(qb * 8 + w) * 2048 + lane * 16;
    bf16x8 q0 = *(const bf16x8*)qp, q1 = *(const bf16x8*)(qp + 1024);
    issue(0); issue(1); issue(2);
    u32x4 stk = *(const u32x4*)(kbase + 3 * 8192 + loff), stv = *(const u32x4*)(vbase + 3 * 8192 + loff);

    f32x16 o = (f32x16){};
    f32x16 negm;
    const LAS char* lp = lds + lane * 16;
    asm volatile("s_waitcnt vmcnt(2) lgkmcnt(0)\n\ts_barrier" ::: "memory");
    DIAG_STAMP(ws, 1, 0);
    bf16x8 kc0 = ldsread16(lp), kc1 = ldsread16(lp + 1024);
    bf16x8 kn0 = ldsread16(lp + 2048), kn1 = ldsread16(lp + 3072);
    bf16x8 vc0 = ldsread16(lp + 8192), vc1 = ldsread16(lp + 8192 + 1024);
    f32x16 sc;
    {
        f32x16 s = MFMA(kc0, q0, (f32x16){}); s = MFMA(kc1, q1, s);
        const float nm = -rowmax16(s);
#pragma unroll
        for (int i = 0; i < 16; ++i) { negm[i] = nm; sc[i] = s[i] + nm; }
    }
    for (int c = 0; c < 16; ++c) {
        const LAS char* cb = lp + (c & (A_NSLOT - 1)) * A_CHUNK;
        const LAS char* nb = lp + ((c + 1) & (A_NSLOT - 1)) * A_CHUNK;
#pragma unroll
        for (int t = 0; t < 4; ++t) {
            if (t == 2) {
                asm volatile("s_waitcnt lgkmcnt(0)\n\ts_barrier" ::: "memory");
                if (c + 3 < 16) {
                    LAS char* wp = (LAS char*)lds_ + ((c + 3) & (A_NSLOT - 1)) * A_CHUNK + loff;
                    *(LAS u32x4*)wp = stk; *(LAS u32x4*)(wp + 8192) = stv;
                    if (c + 4 < 16) { stk = *(const u32x4*)(kbase + (c + 4) * 8192 + loff); stv = *(const u32x4*)(vbase + (c + 4) * 8192 + loff); }
                }
            }
            const LAS char* kp = (t < 2) ? cb + (t + 2) * 2048 : nb + (t - 2) * 2048;
            const LAS char* vp = (t < 3) ? cb + 8192 + (t + 1) * 2048 : nb + 8192;
            EXP4(sc, 0); SB();
            f32x16 sn = MFMA(kn0, q0, negm); PIN(sn); SB();
            EXP4(sc, 4);
            u32x4 pa = {cvtpk(sc[0], sc[1]), cvtpk(sc[2], sc[3]), cvtpk(sc[4], sc[5]), cvtpk(sc[6], sc[7])}; PIN(pa); SB();
            o = MFMA(vc0, __builtin_bit_cast(bf16x8, pa), o); PIN(o); SB();
            bf16x8 kx0 = ldsread16(kp), kx1 = ldsread16(kp + 1024);
            EXP4(sc, 8); SB();
            sn = MFMA(kn1, q1, sn); PIN(sn); SB();
            bf16x8 vn0 = ldsread16(vp), vn1 = ldsread16(vp + 1024);
            EXP4(sc, 12);
            u32x4 pb = {cvtpk(sc[8], sc[9]), cvtpk(sc[10], sc[11]), cvtpk(sc[12], sc[13]), cvtpk(sc[14], sc[15])}; PIN(pb); SB();
            o = MFMA(vc1, __builtin_bit_cast(bf16x8, pb), o); PIN(o); SB();
            sc = sn; vc0 = vn0; vc1 = vn1; kn0 = kx0; kn1 = kx1;
        }
        DIAG_STAMP(ws, 1, c + 1);
    }
    {
        float amax = 0.f; bool bad = false;
#pragma unroll
        for (int i = 0; i < 16; ++i) { const float a = fabsf(o[i]); bad |= !(a < 1.0e30f); amax = fmaxf(amax, a); }
        if (__any(bad)) attn_slow(kbase + lane * 16, vbase + lane * 16, q0, q1, o);
    }
    const int h = lane >> 5, r = lane & 31;
    const float l = __shfl(o[12], r + 32);
    const float inv = 1.0f / l;
    const int blk = bh >> 4, head = bh & 15;
    char* op = ws + WS_O + (size_t)(blk * 64 + qb * 8 + w) * 28672 + r * 16;
#pragma unroll
    for (int gg = 0; gg < 4; ++gg) {
        u32x2 v = {cvtpk(o[4 * gg] * inv, o[4 * gg + 1] * inv), cvtpk(o[4 * gg + 2] * inv, o[4 * gg + 3] * inv)};
        const int k = head * DH + 8 * gg + 4 * h;
        if (gg < 3 || h == 0) *(u32x2*)(op + (k >> 3) * 512 + (k & 7) * 2) = v;
    }
    DIAG_END(ws, 1);
}

__global__ __launch_bounds__(256, 1) void oproj_kernel(const u16* __restrict__ woA, const u16* __restrict__ woB,
                                                       const char* __restrict__ ws, u16* __restrict__ out) {
    DIAG_BEGIN();
    __shared__ __attribute__((aligned(16))) char lds_[448 * 128];
    const LAS char* lds = (const LAS char*)lds_;
    const unsigned lds0 = (unsigned)(uintptr_t)lds_;
    const int tid = threadIdx.x, lane = tid & 63;
    const int w = __builtin_amdgcn_readfirstlane(tid >> 6);
    const int bid = blockIdx.x;
    const int g = bid & 7, rr = bid >> 3;
    const int blk = g >> 2, tg = (g & 3) * 4 + (rr & 3), ng = rr >> 2;
    const u16* wo = blk ? woB : woA;
    const char* wsrc = (const char*)(wo + ng * 64);
#pragma unroll
    for (int j = 0; j < 14; ++j) {
        const int ci = (j * 4 + w) * 64 + lane, k = ci >> 3, slot = ci & 7, ch = slot ^ (((k >> 1) & 1) << 2);
        glds16(wsrc, (unsigned)(k * DM * 2 + ch * 16), lds0 + (j * 4 + w) * 1024);
    }
    const int h = lane >> 5, r = lane & 31;
    const char* ap = ws + WS_O + (size_t)(blk * 64 + tg * 4 + w) * 28672 + lane * 16;
    bf16x8 of[28];
#pragma unroll
    for (int ks = 0; ks < 28; ++ks) of[ks] = *(const bf16x8*)(ap + ks * 1024);
    DIAG_STAMP(ws, 2, 0);
    asm volatile("s_waitcnt vmcnt(28) lgkmcnt(0)\n\ts_barrier" ::: "memory");
    DIAG_STAMP(ws, 2, 1);
    const int i16 = lane & 15, gq = (lane >> 4) & 1, q = i16 >> 2;
    const LAS char* wp0 = lds + (8 * h + q) * 128 + (((q >> 1) & 1) * 64) + (16 * gq + 4 * (i16 & 3)) * 2;
    const LAS char* wp1 = lds + (8 * h + q) * 128 + ((1 ^ ((q >> 1) & 1)) * 64) + (16 * gq + 4 * (i16 & 3)) * 2;
    f32x16 acc0 = (f32x16){}, acc1 = (f32x16){};
    bf16x8 wa[2], wb[2];
    wa[0] = cat8(trread(wp0), trread(wp0 + 512)); wb[0] = cat8(trread(wp1), trread(wp1 + 512));
#pragma unroll
    for (int ks = 0; ks < 28; ++ks) {
        const int cur = ks & 1;
        if (ks + 1 < 28) {
            wa[cur ^ 1] = cat8(trread(wp0 + (ks + 1) * 2048), trread(wp0 + (ks + 1) * 2048 + 512));
            wb[cur ^ 1] = cat8(trread(wp1 + (ks + 1) * 2048), trread(wp1 + (ks + 1) * 2048 + 512));
        }
        SB();
        acc0 = MFMA(wa[cur], of[ks], acc0);
        acc1 = MFMA(wb[cur], of[ks], acc1);
        SB();
    }
    DIAG_STAMP(ws, 2, 2);
    const int tok = tg * 128 + w * 32 + r;
    char* dst = (char*)(out + (size_t)tok * (2 * DM) + blk * DM + ng * 64 + 8 * h);
#pragma unroll
    for (int nt = 0; nt < 2; ++nt) {
        const f32x16& acc = nt ? acc1 : acc0;
        u32x2 pk[4];
#pragma unroll
        for (int gg = 0; gg < 4; ++gg) pk[gg] = (u32x2){cvtpk(acc[4 * gg], acc[4 * gg + 1]), cvtpk(acc[4 * gg + 2], acc[4 * gg + 3])};
#pragma unroll
        for (int gg = 0; gg < 4; gg += 2) {
            auto r0 = __builtin_amdgcn_permlane32_swap(pk[gg][0], pk[gg + 1][0], false, false);
            auto r1 = __builtin_amdgcn_permlane32_swap(pk[gg][1], pk[gg + 1][1], false, false);
            u32x4 v = {r0[0], r1[0], r0[1], r1[1]};
            *(u32x4*)(dst + nt * 64 + gg * 16) = v;
        }
    }
    DIAG_END(ws, 2);
}

extern "C" void kernel_launch(void* const* d_in, const int* in_sizes, int n_in,
                              void* d_out, int out_size, void* d_ws, size_t ws_size,
                              hipStream_t stream) {
    const u16* xA = (const u16*)d_in[0]; const u16* wA = (const u16*)d_in[1]; const u16* woA = (const u16*)d_in[2];
    const u16* xB = (const u16*)d_in[3]; const u16* wB = (const u16*)d_in[4]; const u16* woB = (const u16*)d_in[5];
    char* ws = (char*)d_ws;
    qkv_kernel<<<256, 512, 0, stream>>>(xA, wA, xB, wB, ws);
    attn_kernel<<<256, 512, 0, stream>>>(ws);
    oproj_kernel<<<224, 256, 0, stream>>>(woA, woB, ws, (u16*)d_out);
}
```

```cpp
#include <hip/hip_runtime.h>
#include <hip/hip_bf16.h>
#include <stdint.h>

typedef __attribute__((ext_vector_type(8))) short bf16x8;
typedef __attribute__((ext_vector_type(4))) short s16x4;
typedef __attribute__((ext_vector_type(16))) float f32x16;
typedef __attribute__((ext_vector_type(4))) unsigned u32x4;
typedef __attribute__((ext_vector_type(2))) unsigned u32x2;
typedef unsigned short u16;

#define DI __device__ __forceinline__
#define MFMA(a, b, c) __builtin_amdgcn_mfma_f32_32x32x16_bf16((a), (b), (c), 0, 0, 0)
#define LAS __attribute__((address_space(3)))
#define DIAG_STAMP(wsp, kidx, i)
#define DIAG_BEGIN()
#define DIAG_END(wsp, kidx)

#define SB() __builtin_amdgcn_sched_barrier(0)
#define PIN(x) asm volatile("" : "+v"(x))
constexpr int SEQ = 2048, DM = 448, NH = 16, DH = 28, N3 = 1344;
constexpr size_t FRAG_BYTES = (size_t)2 * NH * 64 * 2048;
constexpr size_t WS_Q = 0, WS_K = FRAG_BYTES, WS_V = 2 * FRAG_BYTES, WS_O = 3 * FRAG_BYTES;
constexpr float QSCALE = 0.18898223650461362f * 1.4426950408889634f;

typedef __bf16 bf16x2_t __attribute__((ext_vector_type(2)));
typedef float f32x2_t __attribute__((ext_vector_type(2)));
DI unsigned cvtpk(float lo, float hi) { f32x2_t v = {lo, hi}; return __builtin_bit_cast(unsigned, __builtin_convertvector(v, bf16x2_t)); }

DI void glds16(const void* sbase, unsigned voff, unsigned lds_base) {
    unsigned sv;
    asm volatile("s_mov_b32 %0, m0\n\ts_mov_b32 m0, %3\n\ts_nop 0\n\tglobal_load_lds_dwordx4 %1, %2\n\ts_mov_b32 m0, %0"
                 : "=&s"(sv) : "v"(voff), "s"(sbase), "s"(lds_base) : "memory");
}
typedef short v4i16_t __attribute__((ext_vector_type(4)));
DI s16x4 trread(const LAS char* p) { return __builtin_bit_cast(s16x4, __builtin_amdgcn_ds_read_tr16_b64_v4i16((LAS v4i16_t*)p)); }
DI bf16x8 ldsread16(const LAS char* p) { return *(const LAS bf16x8*)p; }
DI bf16x8 cat8(s16x4 lo, s16x4 hi) { return __builtin_shufflevector(lo, hi, 0, 1, 2, 3, 4, 5, 6, 7); }

constexpr int Q_SLOT = 40960, Q_NSLOT = 4, Q_WOFF = 16384, Q_WROW = 336;

template <int NQ>
DI void qkv_compute(const LAS char* lds, int xbase0, int wbase, char* ws, int blk, int hs0, int tile0, int lane
) {
    f32x16 acc[6];
#pragma unroll
    for (int i = 0; i < 6; ++i) acc[i] = (f32x16){};
    bf16x8 xa[2][2], wf[2][3];
    auto ldfr = [&](int kk, int buf) {
        const LAS char* sb = lds + ((kk >> 2) % Q_NSLOT) * Q_SLOT; const int ks = kk & 3;
        xa[buf][0] = ldsread16(sb + (xbase0 ^ (ks << 5)));
        xa[buf][1] = ldsread16(sb + ((xbase0 ^ (ks << 5)) + 4096));
#pragma unroll
        for (int i = 0; i < 3; ++i) {
            const LAS char* wp = sb + wbase + ks * 16 * Q_WROW + i * 56;
            wf[buf][i] = cat8(trread(wp), trread(wp + 4 * Q_WROW));
        }
    };
    asm volatile("s_waitcnt lgkmcnt(0)\n\ts_barrier" ::: "memory");
    DIAG_STAMP(ws, 0, 0);
    ldfr(0, 0);
#pragma unroll
    for (int kk = 0; kk < 28; ++kk) {
        const int cur = kk & 1;
        if (kk + 1 < 28) {
            if (((kk + 1) & 3) == 0) { asm volatile("s_waitcnt lgkmcnt(0)\n\ts_barrier" ::: "memory"); DIAG_STAMP(ws, 0, (kk + 1) >> 2); }
            ldfr(kk + 1, cur ^ 1);
        }
        SB();
#pragma unroll
        for (int i = 0; i < 3; ++i) {
            if (i < NQ) { acc[i] = MFMA(wf[cur][i], xa[cur][0], acc[i]); acc[3 + i] = MFMA(wf[cur][i], xa[cur][1], acc[3 + i]); }
            else        { acc[i] = MFMA(xa[cur][0], wf[cur][i], acc[i]); acc[3 + i] = MFMA(xa[cur][1], wf[cur][i], acc[3 + i]); }
        }
        SB();
    }
    DIAG_STAMP(ws, 0, 7);
    const int h = lane >> 5, r = lane & 31;
#pragma unroll
    for (int i = 0; i < 3; ++i) {
        const int hs = hs0 + i, sec = hs >> 4, bh = blk * 16 + (hs & 15);
        const float sc = (sec == 0) ? QSCALE : 1.0f;
#pragma unroll
        for (int st = 0; st < 2; ++st) {
            f32x16 a = acc[st * 3 + i];
            char* dst = ws + (size_t)sec * FRAG_BYTES + ((size_t)(bh * 64 + tile0 + st) * 2) * 1024 + lane * 16;
            if (i < NQ) {
#pragma unroll
                for (int j = 0; j < 16; ++j) a[j] *= sc;
            }
            u32x4 p0 = {cvtpk(a[0], a[1]), cvtpk(a[2], a[3]), cvtpk(a[4], a[5]), cvtpk(a[6], a[7])};
            u32x4 p1 = {cvtpk(a[8], a[9]), cvtpk(a[10], a[11]), cvtpk(a[12], a[13]), cvtpk(a[14], a[15])};
            if (i < NQ) {
                if (h) { p1[2] = (sec == 1) ? 0x00003F80u : 0u; p1[3] = 0u; }
            } else {
                if (r >= 28) { const unsigned f = (r == 28) ? 0x3F803F80u : 0u; p0 = (u32x4){f, f, f, f}; p1 = p0; }
            }
            asm volatile("global_store_dwordx4 %0, %1, off sc1\n\ts_nop 1" :: "v"(dst), "v"(p0) : "memory");
            asm volatile("global_store_dwordx4 %0, %1, off offset:1024 sc1\n\ts_nop 1" :: "v"(dst), "v"(p1) : "memory");
        }
    }
}

__global__ __launch_bounds__(512, 2) void qkv_kernel(const u16* __restrict__ xA, const u16* __restrict__ wA,
                                                     const u16* __restrict__ xB, const u16* __restrict__ wB, char* __restrict__ ws) {
    DIAG_BEGIN();
    __shared__ __attribute__((aligned(16))) char lds_[Q_SLOT * Q_NSLOT];
    const LAS char* lds = (const LAS char*)lds_;
    const unsigned lds0 = (unsigned)(uintptr_t)lds_;
    const int tid = threadIdx.x, lane = tid & 63;
    const int wv = __builtin_amdgcn_readfirstlane(tid >> 6), w = wv & 3;
    const int bid = blockIdx.x, g = bid & 7, rr = bid >> 3;
    const int blk = g >> 2, tt = (g & 1) * 8 + (rr & 7), cg = ((g >> 1) & 1) * 4 + (rr >> 3);
    if (wv >= 4) {
        const u16* x = blk ? xB : xA;
        const u16* wq = blk ? wB : wA;
        const char* xs = (const char*)(x + (size_t)tt * 128 * DM);
        const char* wsrc = (const char*)(wq + cg * 168);
        unsigned xoff;
        { const int row = 8 * w + (lane >> 3), slot = lane & 7, c = slot ^ ((row >> 1) & 7); xoff = (unsigned)(row * DM * 2 + c * 16); }
        unsigned woff[6];
#pragma unroll
        for (int j = 0; j < 6; ++j) {
            int ci = (j * 4 + w) * 64 + lane; if (ci >= 1344) ci -= 1344;
            const int row = ci / 21, ch = ci - row * 21;
            woff[j] = (unsigned)(row * N3 * 2 + ch * 16);
        }
        auto issue = [&](int s) {
            const unsigned sl = lds0 + (unsigned)((s % Q_NSLOT) * Q_SLOT);
            const char* xb = xs + s * 128;
            const char* wb = wsrc + (size_t)s * 64 * N3 * 2;
#pragma unroll
            for (int j = 0; j < 4; ++j) glds16(xb + (size_t)j * 32 * DM * 2, xoff, sl + (j * 4 + w) * 1024);
#pragma unroll
            for (int j = 0; j < 6; ++j) glds16(wb, woff[j], sl + Q_WOFF + (j * 4 + w) * 1024);
        };
        issue(0); issue(1); issue(2);
#pragma unroll
        for (int s = 0; s < 7; ++s) {
            if (s < 5)       asm volatile("s_waitcnt vmcnt(20)\n\ts_barrier" ::: "memory");
            else if (s == 5) asm volatile("s_waitcnt vmcnt(10)\n\ts_barrier" ::: "memory");
            else             asm volatile("s_waitcnt vmcnt(0)\n\ts_barrier" ::: "memory");
            if (s + 3 < 7) issue(s + 3);
        }
        return;
    }
    const int h = lane >> 5, r = lane & 31;
    const int tp = w & 1, ht = w >> 1;
    const int xbase0 = (64 * tp + r) * 128 + ((h ^ ((r >> 1) & 7)) << 4);
    const int i16 = lane & 15, gq = (lane >> 4) & 1;
    const int wbase = Q_WOFF + (8 * h + (i16 >> 2)) * Q_WROW + (16 * gq + 4 * (i16 & 3)) * 2 + ht * 168;
    const int hs0 = cg * 6 + ht * 3;
    const int nq = hs0 >= 32 ? 0 : (32 - hs0 >= 3 ? 3 : 32 - hs0);
    const int tile0 = tt * 4 + tp * 2;
#define DG
    if (nq == 3)      qkv_compute<3>(lds, xbase0, wbase, ws, blk, hs0, tile0, lane DG);
    else if (nq == 0) qkv_compute<0>(lds, xbase0, wbase, ws, blk, hs0, tile0, lane DG);
    else              qkv_compute<2>(lds, xbase0, wbase, ws, blk, hs0, tile0, lane DG);
    DIAG_END(ws, 0);
}

constexpr int A_CHUNK = 16384, A_NSLOT = 4;

#define EXP4(v, i) do { v[i] = __builtin_amdgcn_exp2f(v[i]); v[i + 1] = __builtin_amdgcn_exp2f(v[i + 1]); v[i + 2] = __builtin_amdgcn_exp2f(v[i + 2]); v[i + 3] = __builtin_amdgcn_exp2f(v[i + 3]); } while (0)
DI float rowmax16(const f32x16& s) {
    float a = fmaxf(fmaxf(s[0], s[1]), s[2]);
#pragma unroll
    for (int i = 3; i < 15; i += 2) a = fmaxf(fmaxf(a, s[i]), s[i + 1]);
    a = fmaxf(a, s[15]);
    auto rr = __builtin_amdgcn_permlane32_swap(__float_as_uint(a), __float_as_uint(a), false, false);
    return fmaxf(__uint_as_float(rr[0]), __uint_as_float(rr[1]));
}
DI void pack16(const f32x16& p, bf16x8& p0, bf16x8& p1) {
    u32x4 a = {cvtpk(p[0], p[1]), cvtpk(p[2], p[3]), cvtpk(p[4], p[5]), cvtpk(p[6], p[7])};
    u32x4 b = {cvtpk(p[8], p[9]), cvtpk(p[10], p[11]), cvtpk(p[12], p[13]), cvtpk(p[14], p[15])};
    p0 = __builtin_bit_cast(bf16x8, a); p1 = __builtin_bit_cast(bf16x8, b);
}

DI void attn_slow(const char* kg, const char* vg, bf16x8 q0, bf16x8 q1, f32x16& o) {
    float m = -INFINITY;
    o = (f32x16){};
    for (int t = 0; t < 64; ++t) {
        bf16x8 k0 = *(const bf16x8*)(kg + t * 2048), k1 = *(const bf16x8*)(kg + t * 2048 + 1024);
        bf16x8 v0 = *(const bf16x8*)(vg + t * 2048), v1 = *(const bf16x8*)(vg + t * 2048 + 1024);
        f32x16 s = MFMA(k0, q0, (f32x16){}); s = MFMA(k1, q1, s);
        const float mn = fmaxf(m, rowmax16(s));
        const float alpha = (m == -INFINITY) ? 0.f : __builtin_amdgcn_exp2f(m - mn);
        m = mn;
#pragma unroll
        for (int i = 0; i < 16; ++i) { o[i] *= alpha; s[i] = __builtin_amdgcn_exp2f(s[i] - mn); }
        bf16x8 p0, p1; pack16(s, p0, p1);
        o = MFMA(v0, p0, o); o = MFMA(v1, p1, o);
    }
}

DI float bf16_round(float x) { return __uint_as_float(cvtpk(x, x) << 16); }
DI bf16x8 set_ref(bf16x8 q1, float ref, int h) {
    const short nb = (short)(cvtpk(-ref, -ref) & 0xFFFFu);
    if (h) q1[4] = nb;
    return q1;
}
__global__ __launch_bounds__(576, 3) void attn_kernel(char* __restrict__ ws) {
    DIAG_BEGIN();
    __shared__ __attribute__((aligned(16))) char lds_[A_CHUNK * A_NSLOT];
    const LAS char* lds = (const LAS char*)lds_;
    const unsigned lds0 = (unsigned)(uintptr_t)lds_;
    const int tid = threadIdx.x, lane = tid & 63;
    const int w = __builtin_amdgcn_readfirstlane(tid >> 6);
    const int bid = blockIdx.x;
    const int bh = (bid & 7) * 4 + ((bid >> 3) & 3), qb = bid >> 5;
    const char* kbase = ws + WS_K + (size_t)bh * 131072;
    const char* vbase = ws + WS_V + (size_t)bh * 131072;
    if (w == 8) {
        const unsigned goff = (unsigned)(lane * 16);
        auto issue = [&](int c) {
            const unsigned sl = lds0 + (unsigned)((c & (A_NSLOT - 1)) * A_CHUNK);
#pragma unroll
            for (int hf = 0; hf < 2; ++hf)
#pragma unroll
                for (int j = 0; j < 4; ++j) {
                    glds16(kbase + hf * 65536 + c * 4096 + j * 1024, goff, sl + hf * 8192 + j * 1024);
                    glds16(vbase + hf * 65536 + c * 4096 + j * 1024, goff, sl + hf * 8192 + 4096 + j * 1024);
                }
        };
        issue(0); issue(1); issue(2);
        asm volatile("s_waitcnt vmcnt(32)\n\ts_barrier" ::: "memory");
        for (int c = 0; c < 16; ++c) {
            if (c < 14) asm volatile("s_waitcnt vmcnt(16)\n\ts_barrier" ::: "memory");
            else        asm volatile("s_waitcnt vmcnt(0)\n\ts_barrier" ::: "memory");
            if (c + 3 < 16) issue(c + 3);
        }
        asm volatile("s_barrier" ::: "memory");
        asm volatile("s_barrier" ::: "memory");
        return;
    }
    const int rg = w & 3, kh = w >> 2;
    const int h = lane >> 5, r = lane & 31;
    const char* qp = ws + WS_Q + (size_t)bh * 131072 + (size_t)(qb * 8 + rg * 2) * 2048 + lane * 16;
    bf16x8 qa0 = *(const bf16x8*)qp, qa1 = *(const bf16x8*)(qp + 1024), qb0 = *(const bf16x8*)(qp + 2048), qb1 = *(const bf16x8*)(qp + 3072);

    f32x16 oa = (f32x16){}, ob = (f32x16){};
    f32x16 sa, sb;
    const LAS char* lp = lds + kh * 8192 + lane * 16;
    asm volatile("s_waitcnt lgkmcnt(0)\n\ts_barrier" ::: "memory");
    DIAG_STAMP(ws, 1, 0);
    bf16x8 kn0, kn1, vc0, vc1;
    float ma, mb;
    {
        bf16x8 k0 = ldsread16(lp), k1 = ldsread16(lp + 1024);
        kn0 = ldsread16(lp + 2048); kn1 = ldsread16(lp + 3072);
        vc0 = ldsread16(lp + 4096); vc1 = ldsread16(lp + 4096 + 1024);
        sa = MFMA(k0, qa0, (f32x16){}); sa = MFMA(k1, qa1, sa);
        sb = MFMA(k0, qb0, (f32x16){}); sb = MFMA(k1, qb1, sb);
        ma = bf16_round(rowmax16(sa)); mb = bf16_round(rowmax16(sb));
#pragma unroll
        for (int i = 0; i < 16; ++i) { sa[i] -= ma; sb[i] -= mb; }
    }
    const bf16x8 qa1r = set_ref(qa1, ma, h), qb1r = set_ref(qb1, mb, h);
    for (int c = 0; c < 16; ++c) {
        const LAS char* cb = lp + (c & (A_NSLOT - 1)) * A_CHUNK;
        const LAS char* nb = lp + ((c + 1) & (A_NSLOT - 1)) * A_CHUNK;
#pragma unroll
        for (int t = 0; t < 2; ++t) {
            if (t == 0) asm volatile("s_waitcnt lgkmcnt(0)\n\ts_barrier" ::: "memory");
            const LAS char* kp = nb + t * 2048;
            const LAS char* vp = (t == 0) ? cb + 4096 + 2048 : nb + 4096;
            bf16x8 kx0 = ldsread16(kp), kx1 = ldsread16(kp + 1024);
            EXP4(sa, 0); SB();
            f32x16 sna = MFMA(kn0, qa0, (f32x16){}); PIN(sna); SB();
            EXP4(sa, 4);
            u32x4 pp = {cvtpk(sa[0], sa[1]), cvtpk(sa[2], sa[3]), cvtpk(sa[4], sa[5]), cvtpk(sa[6], sa[7])}; PIN(pp); SB();
            oa = MFMA(vc0, __builtin_bit_cast(bf16x8, pp), oa); PIN(oa); SB();
            bf16x8 vn0 = ldsread16(vp), vn1 = ldsread16(vp + 1024);
            EXP4(sa, 8); SB();
            sna = MFMA(kn1, qa1r, sna); PIN(sna); SB();
            EXP4(sa, 12);
            u32x4 pq = {cvtpk(sa[8], sa[9]), cvtpk(sa[10], sa[11]), cvtpk(sa[12], sa[13]), cvtpk(sa[14], sa[15])}; PIN(pq); SB();
            oa = MFMA(vc1, __builtin_bit_cast(bf16x8, pq), oa); PIN(oa); SB();
            EXP4(sb, 0); SB();
            f32x16 snb = MFMA(kn0, qb0, (f32x16){}); PIN(snb); SB();
            EXP4(sb, 4);
            u32x4 pr = {cvtpk(sb[0], sb[1]), cvtpk(sb[2], sb[3]), cvtpk(sb[4], sb[5]), cvtpk(sb[6], sb[7])}; PIN(pr); SB();
            ob = MFMA(vc0, __builtin_bit_cast(bf16x8, pr), ob); PIN(ob); SB();
            EXP4(sb, 8); SB();
            snb = MFMA(kn1, qb1r, snb); PIN(snb); SB();
            EXP4(sb, 12);
            u32x4 ps = {cvtpk(sb[8], sb[9]), cvtpk(sb[10], sb[11]), cvtpk(sb[12], sb[13]), cvtpk(sb[14], sb[15])}; PIN(ps); SB();
            ob = MFMA(vc1, __builtin_bit_cast(bf16x8, ps), ob); PIN(ob); SB();
            sa = sna; sb = snb; vc0 = vn0; vc1 = vn1; kn0 = kx0; kn1 = kx1;
        }
        DIAG_STAMP(ws, 1, c + 1);
    }
    asm volatile("s_waitcnt lgkmcnt(0)\n\ts_barrier" ::: "memory");
    {
        LAS float* xw = (LAS float*)lds_ + w * (17 * 64) + lane;
        const f32x16& give = kh ? oa : ob;
#pragma unroll
        for (int i = 0; i < 16; ++i) xw[i * 64] = give[i];
        xw[16 * 64] = kh ? ma : mb;
    }
    asm volatile("s_waitcnt lgkmcnt(0)\n\ts_barrier" ::: "memory");
    f32x16 o;
    {
        const LAS float* xr = (const LAS float*)lds_ + (w ^ 4) * (17 * 64) + lane;
        const float m1 = kh ? mb : ma, m2 = xr[16 * 64];
        const float m = fmaxf(m1, m2);
        const float f1 = __builtin_amdgcn_exp2f(m1 - m), f2 = __builtin_amdgcn_exp2f(m2 - m);
        const f32x16& mine = kh ? ob : oa;
#pragma unroll
        for (int i = 0; i < 16; ++i) o[i] = mine[i] * f1 + xr[i * 64] * f2;
    }
    {
        bool bad = false;
#pragma unroll
        for (int i = 0; i < 16; ++i) bad |= !(fabsf(o[i]) < 1.0e30f);
        if (__any(bad)) attn_slow(kbase + lane * 16, vbase + lane * 16, kh ? qb0 : qa0, kh ? qb1 : qa1, o);
    }
    const float l = __shfl(o[12], r + 32);
    const float inv = 1.0f / l;
    const int blk = bh >> 4, head = bh & 15;
    char* op = ws + WS_O + (size_t)(blk * 64 + qb * 8 + rg * 2 + kh) * 28672 + r * 16;
#pragma unroll
    for (int gg = 0; gg < 4; ++gg) {
        u32x2 v = {cvtpk(o[4 * gg] * inv, o[4 * gg + 1] * inv), cvtpk(o[4 * gg + 2] * inv, o[4 * gg + 3] * inv)};
        const int k = head * DH + 8 * gg + 4 * h;
        if (gg < 3 || h == 0) *(u32x2*)(op + (k >> 3) * 512 + (k & 7) * 2) = v;
    }
    DIAG_END(ws, 1);
}

__global__ __launch_bounds__(256, 1) void oproj_kernel(const u16* __restrict__ woA, const u16* __restrict__ woB,
                                                       const char* __restrict__ ws, u16* __restrict__ out) {
    DIAG_BEGIN();
    __shared__ __attribute__((aligned(16))) char lds_[448 * 128];
    const LAS char* lds = (const LAS char*)lds_;
    const unsigned lds0 = (unsigned)(uintptr_t)lds_;
    const int tid = threadIdx.x, lane = tid & 63;
    const int w = __builtin_amdgcn_readfirstlane(tid >> 6);
    const int bid = blockIdx.x;
    const int g = bid & 7, rr = bid >> 3;
    const int blk = g >> 2, tg = (g & 3) * 4 + (rr & 3), ng = rr >> 2;
    const u16* wo = blk ? woB : woA;
    const char* wsrc = (const char*)(wo + ng * 64);
#pragma unroll
    for (int j = 0; j < 14; ++j) {
        const int ci = (j * 4 + w) * 64 + lane, k = ci >> 3, slot = ci & 7, ch = slot ^ (((k >> 1) & 1) << 2);
        glds16(wsrc, (unsigned)(k * DM * 2 + ch * 16), lds0 + (j * 4 + w) * 1024);
    }
    const int h = lane >> 5, r = lane & 31;
    const char* ap = ws + WS_O + (size_t)(blk * 64 + tg * 4 + w) * 28672 + lane * 16;
    bf16x8 of[28];
#pragma unroll
    for (int ks = 0; ks < 28; ++ks) of[ks] = *(const bf16x8*)(ap + ks * 1024);
    DIAG_STAMP(ws, 2, 0);
    asm volatile("s_waitcnt vmcnt(28) lgkmcnt(0)\n\ts_barrier" ::: "memory");
    DIAG_STAMP(ws, 2, 1);
    const int i16 = lane & 15, gq = (lane >> 4) & 1, q = i16 >> 2;
    const LAS char* wp0 = lds + (8 * h + q) * 128 + (((q >> 1) & 1) * 64) + (16 * gq + 4 * (i16 & 3)) * 2;
    const LAS char* wp1 = lds + (8 * h + q) * 128 + ((1 ^ ((q >> 1) & 1)) * 64) + (16 * gq + 4 * (i16 & 3)) * 2;
    f32x16 acc0 = (f32x16){}, acc1 = (f32x16){};
    bf16x8 wa[2], wb[2];
    wa[0] = cat8(trread(wp0), trread(wp0 + 512)); wb[0] = cat8(trread(wp1), trread(wp1 + 512));
#pragma unroll
    for (int ks = 0; ks < 28; ++ks) {
        const int cur = ks & 1;
        if (ks + 1 < 28) {
            wa[cur ^ 1] = cat8(trread(wp0 + (ks + 1) * 2048), trread(wp0 + (ks + 1) * 2048 + 512));
            wb[cur ^ 1] = cat8(trread(wp1 + (ks + 1) * 2048), trread(wp1 + (ks + 1) * 2048 + 512));
        }
        SB();
        acc0 = MFMA(wa[cur], of[ks], acc0);
        acc1 = MFMA(wb[cur], of[ks], acc1);
        SB();
    }
    DIAG_STAMP(ws, 2, 2);
    const int tok = tg * 128 + w * 32 + r;
    char* dst = (char*)(out + (size_t)tok * (2 * DM) + blk * DM + ng * 64 + 8 * h);
#pragma unroll
    for (int nt = 0; nt < 2; ++nt) {
        const f32x16& acc = nt ? acc1 : acc0;
        u32x2 pk[4];
#pragma unroll
        for (int gg = 0; gg < 4; ++gg) pk[gg] = (u32x2){cvtpk(acc[4 * gg], acc[4 * gg + 1]), cvtpk(acc[4 * gg + 2], acc[4 * gg + 3])};
#pragma unroll
        for (int gg = 0; gg < 4; gg += 2) {
            auto r0 = __builtin_amdgcn_permlane32_swap(pk[gg][0], pk[gg + 1][0], false, false);
            auto r1 = __builtin_amdgcn_permlane32_swap(pk[gg][1], pk[gg + 1][1], false, false);
            u32x4 v = {r0[0], r1[0], r0[1], r1[1]};
            *(u32x4*)(dst + nt * 64 + gg * 16) = v;
        }
    }
    DIAG_END(ws, 2);
}

extern "C" void kernel_launch(void* const* d_in, const int* in_sizes, int n_in,
                              void* d_out, int out_size, void* d_ws, size_t ws_size,
                              hipStream_t stream) {
    const u16* xA = (const u16*)d_in[0]; const u16* wA = (const u16*)d_in[1]; const u16* woA = (const u16*)d_in[2];
    const u16* xB = (const u16*)d_in[3]; const u16* wB = (const u16*)d_in[4]; const u16* woB = (const u16*)d_in[5];
    char* ws = (char*)d_ws;
    qkv_kernel<<<256, 512, 0, stream>>>(xA, wA, xB, wB, ws);
    attn_kernel<<<256, 576, 0, stream>>>(ws);
    oproj_kernel<<<224, 256, 0, stream>>>(woA, woB, ws, (u16*)d_out);
}
```

```cpp
#include <hip/hip_runtime.h>
#include <hip/hip_bf16.h>
#include <stdint.h>

typedef __attribute__((ext_vector_type(8))) short bf16x8;
typedef __attribute__((ext_vector_type(4))) short s16x4;
typedef __attribute__((ext_vector_type(16))) float f32x16;
typedef __attribute__((ext_vector_type(4))) unsigned u32x4;
typedef __attribute__((ext_vector_type(2))) unsigned u32x2;
typedef unsigned short u16;

#define DI __device__ __forceinline__
#define MFMA(a, b, c) __builtin_amdgcn_mfma_f32_32x32x16_bf16((a), (b), (c), 0, 0, 0)
#define LAS __attribute__((address_space(3)))
#define DIAG_STAMP(wsp, kidx, i)
#define DIAG_BEGIN()
#define DIAG_END(wsp, kidx)

#define SB() __builtin_amdgcn_sched_barrier(0)
#define PIN(x) asm volatile("" : "+v"(x))
constexpr int SEQ = 2048, DM = 448, NH = 16, DH = 28, N3 = 1344;
constexpr size_t FRAG_BYTES = (size_t)2 * NH * 64 * 2048;
constexpr size_t WS_Q = 0, WS_K = FRAG_BYTES, WS_V = 2 * FRAG_BYTES, WS_O = 3 * FRAG_BYTES;
constexpr float QSCALE = 0.18898223650461362f * 1.4426950408889634f;

typedef __bf16 bf16x2_t __attribute__((ext_vector_type(2)));
typedef float f32x2_t __attribute__((ext_vector_type(2)));
DI unsigned cvtpk(float lo, float hi) { f32x2_t v = {lo, hi}; return __builtin_bit_cast(unsigned, __builtin_convertvector(v, bf16x2_t)); }

DI void glds16(const void* sbase, unsigned voff, unsigned lds_base) {
    unsigned sv;
    asm volatile("s_mov_b32 %0, m0\n\ts_mov_b32 m0, %3\n\ts_nop 0\n\tglobal_load_lds_dwordx4 %1, %2\n\ts_mov_b32 m0, %0"
                 : "=&s"(sv) : "v"(voff), "s"(sbase), "s"(lds_base) : "memory");
}
typedef short v4i16_t __attribute__((ext_vector_type(4)));
DI s16x4 trread(const LAS char* p) { return __builtin_bit_cast(s16x4, __builtin_amdgcn_ds_read_tr16_b64_v4i16((LAS v4i16_t*)p)); }
DI bf16x8 ldsread16(const LAS char* p) { return *(const LAS bf16x8*)p; }
DI bf16x8 cat8(s16x4 lo, s16x4 hi) { return __builtin_shufflevector(lo, hi, 0, 1, 2, 3, 4, 5, 6, 7); }

constexpr int Q_SLOT = 40960, Q_NSLOT = 4, Q_WOFF = 16384, Q_WROW = 336;

template <int NQ>
DI void qkv_compute(const LAS char* lds, int xbase0, int wbase, char* ws, int blk, int hs0, int tile0, int lane
) {
    f32x16 acc[6];
#pragma unroll
    for (int i = 0; i < 6; ++i) acc[i] = (f32x16){};
    bf16x8 xa[2][2], wf[2][3];
    auto ldfr = [&](int kk, int buf) {
        const LAS char* sb = lds + ((kk >> 2) % Q_NSLOT) * Q_SLOT; const int ks = kk & 3;
        xa[buf][0] = ldsread16(sb + (xbase0 ^ (ks << 5)));
        xa[buf][1] = ldsread16(sb + ((xbase0 ^ (ks << 5)) + 4096));
#pragma unroll
        for (int i = 0; i < 3; ++i) {
            const LAS char* wp = sb + wbase + ks * 16 * Q_WROW + i * 56;
            wf[buf][i] = cat8(trread(wp), trread(wp + 4 * Q_WROW));
        }
    };
    asm volatile("s_waitcnt lgkmcnt(0)\n\ts_barrier" ::: "memory");
    DIAG_STAMP(ws, 0, 0);
    ldfr(0, 0);
#pragma unroll
    for (int kk = 0; kk < 28; ++kk) {
        const int cur = kk & 1;
        if (kk + 1 < 28) {
            if (((kk + 1) & 3) == 0) { asm volatile("s_waitcnt lgkmcnt(0)\n\ts_barrier" ::: "memory"); DIAG_STAMP(ws, 0, (kk + 1) >> 2); }
            ldfr(kk + 1, cur ^ 1);
        }
        SB();
#pragma unroll
        for (int i = 0; i < 3; ++i) {
            if (i < NQ) { acc[i] = MFMA(wf[cur][i], xa[cur][0], acc[i]); acc[3 + i] = MFMA(wf[cur][i], xa[cur][1], acc[3 + i]); }
            else        { acc[i] = MFMA(xa[cur][0], wf[cur][i], acc[i]); acc[3 + i] = MFMA(xa[cur][1], wf[cur][i], acc[3 + i]); }
        }
        SB();
    }
    DIAG_STAMP(ws, 0, 7);
    const int h = lane >> 5, r = lane & 31;
#pragma unroll
    for (int i = 0; i < 3; ++i) {
        const int hs = hs0 + i, sec = hs >> 4, bh = blk * 16 + (hs & 15);
        const float sc = (sec == 0) ? QSCALE : 1.0f;
#pragma unroll
        for (int st = 0; st < 2; ++st) {
            f32x16 a = acc[st * 3 + i];
            char* dst = ws + (size_t)sec * FRAG_BYTES + ((size_t)(bh * 64 + tile0 + st) * 2) * 1024 + lane * 16;
            if (i < NQ) {
#pragma unroll
                for (int j = 0; j < 16; ++j) a[j] *= sc;
            }
            u32x4 p0 = {cvtpk(a[0], a[1]), cvtpk(a[2], a[3]), cvtpk(a[4], a[5]), cvtpk(a[6], a[7])};
            u32x4 p1 = {cvtpk(a[8], a[9]), cvtpk(a[10], a[11]), cvtpk(a[12], a[13]), cvtpk(a[14], a[15])};
            if (i < NQ) {
                if (h) { p1[2] = (sec == 1) ? 0x00003F80u : 0u; p1[3] = 0u; }
            } else {
                if (r >= 28) { const unsigned f = (r == 28) ? 0x3F803F80u : 0u; p0 = (u32x4){f, f, f, f}; p1 = p0; }
            }
            asm volatile("global_store_dwordx4 %0, %1, off sc1\n\ts_nop 1" :: "v"(dst), "v"(p0) : "memory");
            asm volatile("global_store_dwordx4 %0, %1, off offset:1024 sc1\n\ts_nop 1" :: "v"(dst), "v"(p1) : "memory");
        }
    }
}

__global__ __launch_bounds__(512, 2) void qkv_kernel(const u16* __restrict__ xA, const u16* __restrict__ wA,
                                                     const u16* __restrict__ xB, const u16* __restrict__ wB, char* __restrict__ ws) {
    DIAG_BEGIN();
    __shared__ __attribute__((aligned(16))) char lds_[Q_SLOT * Q_NSLOT];
    const LAS char* lds = (const LAS char*)lds_;
    const unsigned lds0 = (unsigned)(uintptr_t)lds_;
    const int tid = threadIdx.x, lane = tid & 63;
    const int wv = __builtin_amdgcn_readfirstlane(tid >> 6), w = wv & 3;
    const int bid = blockIdx.x, g = bid & 7, rr = bid >> 3;
    const int blk = g >> 2, tt = (g & 1) * 8 + (rr & 7), cg = ((g >> 1) & 1) * 4 + (rr >> 3);
    if (wv >= 4) {
        const u16* x = blk ? xB : xA;
        const u16* wq = blk ? wB : wA;
        const char* xs = (const char*)(x + (size_t)tt * 128 * DM);
        const char* wsrc = (const char*)(wq + cg * 168);
        unsigned xoff;
        { const int row = 8 * w + (lane >> 3), slot = lane & 7, c = slot ^ ((row >> 1) & 7); xoff = (unsigned)(row * DM * 2 + c * 16); }
        unsigned woff[6];
#pragma unroll
        for (int j = 0; j < 6; ++j) {
            int ci = (j * 4 + w) * 64 + lane; if (ci >= 1344) ci -= 1344;
            const int row = ci / 21, ch = ci - row * 21;
            woff[j] = (unsigned)(row * N3 * 2 + ch * 16);
        }
        auto issue = [&](int s) {
            const unsigned sl = lds0 + (unsigned)((s % Q_NSLOT) * Q_SLOT);
            const char* xb = xs + s * 128;
            const char* wb = wsrc + (size_t)s * 64 * N3 * 2;
#pragma unroll
            for (int j = 0; j < 4; ++j) glds16(xb + (size_t)j * 32 * DM * 2, xoff, sl + (j * 4 + w) * 1024);
#pragma unroll
            for (int j = 0; j < 6; ++j) glds16(wb, woff[j], sl + Q_WOFF + (j * 4 + w) * 1024);
        };
        issue(0); issue(1); issue(2);
#pragma unroll
        for (int s = 0; s < 7; ++s) {
            if (s < 5)       asm volatile("s_waitcnt vmcnt(20)\n\ts_barrier" ::: "memory");
            else if (s == 5) asm volatile("s_waitcnt vmcnt(10)\n\ts_barrier" ::: "memory");
            else             asm volatile("s_waitcnt vmcnt(0)\n\ts_barrier" ::: "memory");
            if (s + 3 < 7) issue(s + 3);
        }
        return;
    }
    const int h = lane >> 5, r = lane & 31;
    const int tp = w & 1, ht = w >> 1;
    const int xbase0 = (64 * tp + r) * 128 + ((h ^ ((r >> 1) & 7)) << 4);
    const int i16 = lane & 15, gq = (lane >> 4) & 1;
    const int wbase = Q_WOFF + (8 * h + (i16 >> 2)) * Q_WROW + (16 * gq + 4 * (i16 & 3)) * 2 + ht * 168;
    const int hs0 = cg * 6 + ht * 3;
    const int nq = hs0 >= 32 ? 0 : (32 - hs0 >= 3 ? 3 : 32 - hs0);
    const int tile0 = tt * 4 + tp * 2;
#define DG
    if (nq == 3)      qkv_compute<3>(lds, xbase0, wbase, ws, blk, hs0, tile0, lane DG);
    else if (nq == 0) qkv_compute<0>(lds, xbase0, wbase, ws, blk, hs0, tile0, lane DG);
    else              qkv_compute<2>(lds, xbase0, wbase, ws, blk, hs0, tile0, lane DG);
    DIAG_END(ws, 0);
}

constexpr int A_CHUNK = 16384, A_NSLOT = 4;

#define EXP4(v, i) do { v[i] = __builtin_amdgcn_exp2f(v[i]); v[i + 1] = __builtin_amdgcn_exp2f(v[i + 1]); v[i + 2] = __builtin_amdgcn_exp2f(v[i + 2]); v[i + 3] = __builtin_amdgcn_exp2f(v[i + 3]); } while (0)
DI float rowmax16(const f32x16& s) {
    float a = fmaxf(fmaxf(s[0], s[1]), s[2]);
#pragma unroll
    for (int i = 3; i < 15; i += 2) a = fmaxf(fmaxf(a, s[i]), s[i + 1]);
    a = fmaxf(a, s[15]);
    auto rr = __builtin_amdgcn_permlane32_swap(__float_as_uint(a), __float_as_uint(a), false, false);
    return fmaxf(__uint_as_float(rr[0]), __uint_as_float(rr[1]));
}
DI void pack16(const f32x16& p, bf16x8& p0, bf16x8& p1) {
    u32x4 a = {cvtpk(p[0], p[1]), cvtpk(p[2], p[3]), cvtpk(p[4], p[5]), cvtpk(p[6], p[7])};
    u32x4 b = {cvtpk(p[8], p[9]), cvtpk(p[10], p[11]), cvtpk(p[12], p[13]), cvtpk(p[14], p[15])};
    p0 = __builtin_bit_cast(bf16x8, a); p1 = __builtin_bit_cast(bf16x8, b);
}

DI void attn_slow(const char* kg, const char* vg, bf16x8 q0, bf16x8 q1, f32x16& o) {
    float m = -INFINITY;
    o = (f32x16){};
    for (int t = 0; t < 64; ++t) {
        bf16x8 k0 = *(const bf16x8*)(kg + t * 2048), k1 = *(const bf16x8*)(kg + t * 2048 + 1024);
        bf16x8 v0 = *(const bf16x8*)(vg + t * 2048), v1 = *(const bf16x8*)(vg + t * 2048 + 1024);
        f32x16 s = MFMA(k0, q0, (f32x16){}); s = MFMA(k1, q1, s);
        const float mn = fmaxf(m, rowmax16(s));
        const float alpha = (m == -INFINITY) ? 0.f : __builtin_amdgcn_exp2f(m - mn);
        m = mn;
#pragma unroll
        for (int i = 0; i < 16; ++i) { o[i] *= alpha; s[i] = __builtin_amdgcn_exp2f(s[i] - mn); }
        bf16x8 p0, p1; pack16(s, p0, p1);
        o = MFMA(v0, p0, o); o = MFMA(v1, p1, o);
    }
}

DI float bf16_round(float x) { return __uint_as_float(cvtpk(x, x) << 16); }
DI bf16x8 set_ref(bf16x8 q1, float ref, int h) {
    const short nb = (short)(cvtpk(-ref, -ref) & 0xFFFFu);
    if (h) q1[4] = nb;
    return q1;
}
__global__ __launch_bounds__(640, 3) void attn_kernel(char* __restrict__ ws, const u16* __restrict__ woA, const u16* __restrict__ woB) {
    DIAG_BEGIN();
    __shared__ __attribute__((aligned(16))) char lds_[A_CHUNK * A_NSLOT];
    const LAS char* lds = (const LAS char*)lds_;
    const unsigned lds0 = (unsigned)(uintptr_t)lds_;
    const int tid = threadIdx.x, lane = tid & 63;
    const int w = __builtin_amdgcn_readfirstlane(tid >> 6);
    const int bid = blockIdx.x;
    const int bh = (bid & 7) * 4 + ((bid >> 3) & 3), qb = bid >> 5;
    const char* kbase = ws + WS_K + (size_t)bh * 131072;
    const char* vbase = ws + WS_V + (size_t)bh * 131072;
    if (w >= 8) {
        const int hf = w - 8;
        const unsigned goff = (unsigned)(lane * 16);
        auto issue = [&](int c) {
            const unsigned sl = lds0 + (unsigned)((c & (A_NSLOT - 1)) * A_CHUNK) + hf * 8192;
#pragma unroll
            for (int j = 0; j < 4; ++j) {
                glds16(kbase + hf * 65536 + c * 4096 + j * 1024, goff, sl + j * 1024);
                glds16(vbase + hf * 65536 + c * 4096 + j * 1024, goff, sl + 4096 + j * 1024);
            }
        };
        issue(1); issue(2);
        unsigned pf;
        {
            const char* wp = (const char*)(hf ? woB : woA) + (size_t)bid * 1568 + (lane < 13 ? lane * 128 : 0);
            asm volatile("global_load_dword %0, %1, off" : "=v"(pf) : "v"(wp) : "memory");
        }
        asm volatile("s_barrier" ::: "memory");
        for (int c = 0; c < 16; ++c) {
            if (c < 2)       asm volatile("s_waitcnt vmcnt(9)\n\ts_barrier" ::: "memory");
            else if (c < 14) asm volatile("s_waitcnt vmcnt(8)\n\ts_barrier" ::: "memory");
            else             asm volatile("s_waitcnt vmcnt(0)\n\ts_barrier" ::: "memory");
            if (c + 3 < 16) issue(c + 3);
        }
        asm volatile("" :: "v"(pf));
        asm volatile("s_barrier" ::: "memory");
        asm volatile("s_barrier" ::: "memory");
        return;
    }
    const int rg = w & 3, kh = w >> 2;
    const int h = lane >> 5, r = lane & 31;
    const char* qp = ws + WS_Q + (size_t)bh * 131072 + (size_t)(qb * 8 + rg * 2) * 2048 + lane * 16;
    glds16(kbase + kh * 65536 + rg * 1024, (unsigned)(lane * 16), lds0 + kh * 8192 + rg * 1024);
    glds16(vbase + kh * 65536 + rg * 1024, (unsigned)(lane * 16), lds0 + kh * 8192 + 4096 + rg * 1024);
    bf16x8 qa0 = *(const bf16x8*)qp, qa1 = *(const bf16x8*)(qp + 1024), qb0 = *(const bf16x8*)(qp + 2048), qb1 = *(const bf16x8*)(qp + 3072);

    f32x16 oa = (f32x16){}, ob = (f32x16){};
    f32x16 sa, sb;
    const LAS char* lp = lds + kh * 8192 + lane * 16;
    asm volatile("s_waitcnt vmcnt(0) lgkmcnt(0)\n\ts_barrier" ::: "memory");
    DIAG_STAMP(ws, 1, 0);
    bf16x8 kn0, kn1, vc0, vc1;
    float ma, mb;
    {
        bf16x8 k0 = ldsread16(lp), k1 = ldsread16(lp + 1024);
        kn0 = ldsread16(lp + 2048); kn1 = ldsread16(lp + 3072);
        vc0 = ldsread16(lp + 4096); vc1 = ldsread16(lp + 4096 + 1024);
        sa = MFMA(k0, qa0, (f32x16){}); sa = MFMA(k1, qa1, sa);
        sb = MFMA(k0, qb0, (f32x16){}); sb = MFMA(k1, qb1, sb);
        ma = bf16_round(rowmax16(sa)); mb = bf16_round(rowmax16(sb));
#pragma unroll
        for (int i = 0; i < 16; ++i) { sa[i] -= ma; sb[i] -= mb; }
    }
    const bf16x8 qa1r = set_ref(qa1, ma, h), qb1r = set_ref(qb1, mb, h);
    for (int c = 0; c < 16; ++c) {
        const LAS char* cb = lp + (c & (A_NSLOT - 1)) * A_CHUNK;
        const LAS char* nb = lp + ((c + 1) & (A_NSLOT - 1)) * A_CHUNK;
#pragma unroll
        for (int t = 0; t < 2; ++t) {
            if (t == 0) asm volatile("s_waitcnt lgkmcnt(0)\n\ts_barrier" ::: "memory");
            const LAS char* kp = nb + t * 2048;
            const LAS char* vp = (t == 0) ? cb + 4096 + 2048 : nb + 4096;
            bf16x8 kx0 = ldsread16(kp), kx1 = ldsread16(kp + 1024);
            EXP4(sa, 0); SB();
            f32x16 sna = MFMA(kn0, qa0, (f32x16){}); PIN(sna); SB();
            EXP4(sa, 4);
            u32x4 pp = {cvtpk(sa[0], sa[1]), cvtpk(sa[2], sa[3]), cvtpk(sa[4], sa[5]), cvtpk(sa[6], sa[7])}; PIN(pp); SB();
            oa = MFMA(vc0, __builtin_bit_cast(bf16x8, pp), oa); PIN(oa); SB();
            bf16x8 vn0 = ldsread16(vp), vn1 = ldsread16(vp + 1024);
            EXP4(sa, 8); SB();
            sna = MFMA(kn1, qa1r, sna); PIN(sna); SB();
            EXP4(sa, 12);
            u32x4 pq = {cvtpk(sa[8], sa[9]), cvtpk(sa[10], sa[11]), cvtpk(sa[12], sa[13]), cvtpk(sa[14], sa[15])}; PIN(pq); SB();
            oa = MFMA(vc1, __builtin_bit_cast(bf16x8, pq), oa); PIN(oa); SB();
            EXP4(sb, 0); SB();
            f32x16 snb = MFMA(kn0, qb0, (f32x16){}); PIN(snb); SB();
            EXP4(sb, 4);
            u32x4 pr = {cvtpk(sb[0], sb[1]), cvtpk(sb[2], sb[3]), cvtpk(sb[4], sb[5]), cvtpk(sb[6], sb[7])}; PIN(pr); SB();
            ob = MFMA(vc0, __builtin_bit_cast(bf16x8, pr), ob); PIN(ob); SB();
            EXP4(sb, 8); SB();
            snb = MFMA(kn1, qb1r, snb); PIN(snb); SB();
            EXP4(sb, 12);
            u32x4 ps = {cvtpk(sb[8], sb[9]), cvtpk(sb[10], sb[11]), cvtpk(sb[12], sb[13]), cvtpk(sb[14], sb[15])}; PIN(ps); SB();
            ob = MFMA(vc1, __builtin_bit_cast(bf16x8, ps), ob); PIN(ob); SB();
            sa = sna; sb = snb; vc0 = vn0; vc1 = vn1; kn0 = kx0; kn1 = kx1;
        }
        DIAG_STAMP(ws, 1, c + 1);
    }
    asm volatile("s_waitcnt lgkmcnt(0)\n\ts_barrier" ::: "memory");
    {
        LAS float* xw = (LAS float*)lds_ + w * (17 * 64) + lane;
        const f32x16& give = kh ? oa : ob;
#pragma unroll
        for (int i = 0; i < 16; ++i) xw[i * 64] = give[i];
        xw[16 * 64] = kh ? ma : mb;
    }
    asm volatile("s_waitcnt lgkmcnt(0)\n\ts_barrier" ::: "memory");
    f32x16 o;
    {
        const LAS float* xr = (const LAS float*)lds_ + (w ^ 4) * (17 * 64) + lane;
        const float m1 = kh ? mb : ma, m2 = xr[16 * 64];
        const float m = fmaxf(m1, m2);
        const float f1 = __builtin_amdgcn_exp2f(m1 - m), f2 = __builtin_amdgcn_exp2f(m2 - m);
        const f32x16& mine = kh ? ob : oa;
#pragma unroll
        for (int i = 0; i < 16; ++i) o[i] = mine[i] * f1 + xr[i * 64] * f2;
    }
    {
        bool bad = false;
#pragma unroll
        for (int i = 0; i < 16; ++i) bad |= !(fabsf(o[i]) < 1.0e30f);
        if (__any(bad)) attn_slow(kbase + lane * 16, vbase + lane * 16, kh ? qb0 : qa0, kh ? qb1 : qa1, o);
    }
    const float l = __shfl(o[12], r + 32);
    const float inv = 1.0f / l;
    const int blk = bh >> 4, head = bh & 15;
    char* op = ws + WS_O + (size_t)(blk * 64 + qb * 8 + rg * 2 + kh) * 28672 + r * 16;
#pragma unroll
    for (int gg = 0; gg < 4; ++gg) {
        u32x2 v = {cvtpk(o[4 * gg] * inv, o[4 * gg + 1] * inv), cvtpk(o[4 * gg + 2] * inv, o[4 * gg + 3] * inv)};
        const int k = head * DH + 8 * gg + 4 * h;
        if (gg < 3 || h == 0) *(u32x2*)(op + (k >> 3) * 512 + (k & 7) * 2) = v;
    }
    DIAG_END(ws, 1);
}

__global__ __launch_bounds__(256, 1) void oproj_kernel(const u16* __restrict__ woA, const u16* __restrict__ woB,
                                                       const char* __restrict__ ws, u16* __restrict__ out) {
    DIAG_BEGIN();
    __shared__ __attribute__((aligned(16))) char lds_[448 * 128];
    const LAS char* lds = (const LAS char*)lds_;
    const unsigned lds0 = (unsigned)(uintptr_t)lds_;
    const int tid = threadIdx.x, lane = tid & 63;
    const int w = __builtin_amdgcn_readfirstlane(tid >> 6);
    const int bid = blockIdx.x;
    const int g = bid & 7, rr = bid >> 3;
    const int blk = g >> 2, tg = (g & 3) * 4 + (rr & 3), ng = rr >> 2;
    const u16* wo = blk ? woB : woA;
    const char* wsrc = (const char*)(wo + ng * 64);
#pragma unroll
    for (int j = 0; j < 14; ++j) {
        const int ci = (j * 4 + w) * 64 + lane, k = ci >> 3, slot = ci & 7, ch = slot ^ (((k >> 1) & 1) << 2);
        glds16(wsrc, (unsigned)(k * DM * 2 + ch * 16), lds0 + (j * 4 + w) * 1024);
    }
    const int h = lane >> 5, r = lane & 31;
    const char* ap = ws + WS_O + (size_t)(blk * 64 + tg * 4 + w) * 28672 + lane * 16;
    bf16x8 of[28];
#pragma unroll
    for (int ks = 0; ks < 28; ++ks) of[ks] = *(const bf16x8*)(ap + ks * 1024);
    DIAG_STAMP(ws, 2, 0);
    asm volatile("s_waitcnt vmcnt(28) lgkmcnt(0)\n\ts_barrier" ::: "memory");
    DIAG_STAMP(ws, 2, 1);
    const int i16 = lane & 15, gq = (lane >> 4) & 1, q = i16 >> 2;
    const LAS char* wp0 = lds + (8 * h + q) * 128 + (((q >> 1) & 1) * 64) + (16 * gq + 4 * (i16 & 3)) * 2;
    const LAS char* wp1 = lds + (8 * h + q) * 128 + ((1 ^ ((q >> 1) & 1)) * 64) + (16 * gq + 4 * (i16 & 3)) * 2;
    f32x16 acc0 = (f32x16){}, acc1 = (f32x16){};
    bf16x8 wa[2], wb[2];
    wa[0] = cat8(trread(wp0), trread(wp0 + 512)); wb[0] = cat8(trread(wp1), trread(wp1 + 512));
#pragma unroll
    for (int ks = 0; ks < 28; ++ks) {
        const int cur = ks & 1;
        if (ks + 1 < 28) {
            wa[cur ^ 1] = cat8(trread(wp0 + (ks + 1) * 2048), trread(wp0 + (ks + 1) * 2048 + 512));
            wb[cur ^ 1] = cat8(trread(wp1 + (ks + 1) * 2048), trread(wp1 + (ks + 1) * 2048 + 512));
        }
        SB();
        acc0 = MFMA(wa[cur], of[ks], acc0);
        acc1 = MFMA(wb[cur], of[ks], acc1);
        SB();
    }
    DIAG_STAMP(ws, 2, 2);
    const int tok = tg * 128 + w * 32 + r;
    char* dst = (char*)(out + (size_t)tok * (2 * DM) + blk * DM + ng * 64 + 8 * h);
#pragma unroll
    for (int nt = 0; nt < 2; ++nt) {
        const f32x16& acc = nt ? acc1 : acc0;
        u32x2 pk[4];
#pragma unroll
        for (int gg = 0; gg < 4; ++gg) pk[gg] = (u32x2){cvtpk(acc[4 * gg], acc[4 * gg + 1]), cvtpk(acc[4 * gg + 2], acc[4 * gg + 3])};
#pragma unroll
        for (int gg = 0; gg < 4; gg += 2) {
            auto r0 = __builtin_amdgcn_permlane32_swap(pk[gg][0], pk[gg + 1][0], false, false);
            auto r1 = __builtin_amdgcn_permlane32_swap(pk[gg][1], pk[gg + 1][1], false, false);
            u32x4 v = {r0[0], r1[0], r0[1], r1[1]};
            *(u32x4*)(dst + nt * 64 + gg * 16) = v;
        }
    }
    DIAG_END(ws, 2);
}

extern "C" void kernel_launch(void* const* d_in, const int* in_sizes, int n_in,
                              void* d_out, int out_size, void* d_ws, size_t ws_size,
                              hipStream_t stream) {
    const u16* xA = (const u16*)d_in[0]; const u16* wA = (const u16*)d_in[1]; const u16* woA = (const u16*)d_in[2];
    const u16* xB = (const u16*)d_in[3]; const u16* wB = (const u16*)d_in[4]; const u16* woB = (const u16*)d_in[5];
    char* ws = (char*)d_ws;
    qkv_kernel<<<256, 512, 0, stream>>>(xA, wA, xB, wB, ws);
    attn_kernel<<<256, 640, 0, stream>>>(ws, woA, woB);
    oproj_kernel<<<224, 256, 0, stream>>>(woA, woB, ws, (u16*)d_out);
}
```

```cpp
#include <hip/hip_runtime.h>
#include <hip/hip_bf16.h>
#include <stdint.h>

typedef __attribute__((ext_vector_type(8))) short bf16x8;
typedef __attribute__((ext_vector_type(4))) short s16x4;
typedef __attribute__((ext_vector_type(16))) float f32x16;
typedef __attribute__((ext_vector_type(4))) unsigned u32x4;
typedef __attribute__((ext_vector_type(2))) unsigned u32x2;
typedef unsigned short u16;

#define DI __device__ __forceinline__
#define MFMA(a, b, c) __builtin_amdgcn_mfma_f32_32x32x16_bf16((a), (b), (c), 0, 0, 0)
#define LAS __attribute__((address_space(3)))
#define SB() __builtin_amdgcn_sched_barrier(0)
#define PIN(x) asm volatile("" : "+v"(x))
constexpr int SEQ = 2048, DM = 448, NH = 16, DH = 28, N3 = 1344;
constexpr size_t FRAG_BYTES = (size_t)2 * NH * 64 * 2048;
constexpr size_t WS_Q = 0, WS_K = FRAG_BYTES, WS_V = 2 * FRAG_BYTES, WS_O = 3 * FRAG_BYTES;
constexpr float QSCALE = 0.18898223650461362f * 1.4426950408889634f;

typedef __bf16 bf16x2_t __attribute__((ext_vector_type(2)));
typedef float f32x2_t __attribute__((ext_vector_type(2)));
DI unsigned cvtpk(float lo, float hi) { f32x2_t v = {lo, hi}; return __builtin_bit_cast(unsigned, __builtin_convertvector(v, bf16x2_t)); }

DI void glds16(const void* sbase, unsigned voff, unsigned lds_base) {
    unsigned sv;
    asm volatile("s_mov_b32 %0, m0\n\ts_mov_b32 m0, %3\n\ts_nop 0\n\tglobal_load_lds_dwordx4 %1, %2\n\ts_mov_b32 m0, %0"
                 : "=&s"(sv) : "v"(voff), "s"(sbase), "s"(lds_base) : "memory");
}
typedef short v4i16_t __attribute__((ext_vector_type(4)));
DI s16x4 trread(const LAS char* p) { return __builtin_bit_cast(s16x4, __builtin_amdgcn_ds_read_tr16_b64_v4i16((LAS v4i16_t*)p)); }
DI bf16x8 ldsread16(const LAS char* p) { return *(const LAS bf16x8*)p; }
DI bf16x8 cat8(s16x4 lo, s16x4 hi) { return __builtin_shufflevector(lo, hi, 0, 1, 2, 3, 4, 5, 6, 7); }

constexpr int Q_SLOT = 40960, Q_NSLOT = 4, Q_WOFF = 16384, Q_WROW = 336;

template <int NQ>
DI void qkv_compute(const LAS char* lds, int xbase0, int wbase, char* ws, int blk, int hs0, int tile0, int lane) {
    f32x16 acc[6];
#pragma unroll
    for (int i = 0; i < 6; ++i) acc[i] = (f32x16){};
    bf16x8 xa[2][2], wf[2][3];
    auto ldfr = [&](int kk, int buf) {
        const LAS char* sb = lds + ((kk >> 2) % Q_NSLOT) * Q_SLOT; const int ks = kk & 3;
        xa[buf][0] = ldsread16(sb + (xbase0 ^ (ks << 5)));
        xa[buf][1] = ldsread16(sb + ((xbase0 ^ (ks << 5)) + 4096));
#pragma unroll
        for (int i = 0; i < 3; ++i) {
            const LAS char* wp = sb + wbase + ks * 16 * Q_WROW + i * 56;
            wf[buf][i] = cat8(trread(wp), trread(wp + 4 * Q_WROW));
        }
    };
    asm volatile("s_waitcnt lgkmcnt(0)\n\ts_barrier" ::: "memory");
    ldfr(0, 0);
#pragma unroll
    for (int kk = 0; kk < 28; ++kk) {
        const int cur = kk & 1;
        if (kk + 1 < 28) {
            if (((kk + 1) & 3) == 0) { asm volatile("s_waitcnt lgkmcnt(0)\n\ts_barrier" ::: "memory"); }
            ldfr(kk + 1, cur ^ 1);
        }
        SB();
#pragma unroll
        for (int i = 0; i < 3; ++i) {
            if (i < NQ) { acc[i] = MFMA(wf[cur][i], xa[cur][0], acc[i]); acc[3 + i] = MFMA(wf[cur][i], xa[cur][1], acc[3 + i]); }
            else        { acc[i] = MFMA(xa[cur][0], wf[cur][i], acc[i]); acc[3 + i] = MFMA(xa[cur][1], wf[cur][i], acc[3 + i]); }
        }
        SB();
    }
    const int h = lane >> 5, r = lane & 31;
#pragma unroll
    for (int i = 0; i < 3; ++i) {
        const int hs = hs0 + i, sec = hs >> 4, bh = blk * 16 + (hs & 15);
        const float sc = (sec == 0) ? QSCALE : 1.0f;
#pragma unroll
        for (int st = 0; st < 2; ++st) {
            f32x16 a = acc[st * 3 + i];
            char* dst = ws + (size_t)sec * FRAG_BYTES + ((size_t)(bh * 64 + tile0 + st) * 2) * 1024 + lane * 16;
            if (i < NQ) {
#pragma unroll
                for (int j = 0; j < 16; ++j) a[j] *= sc;
            }
            u32x4 p0 = {cvtpk(a[0], a[1]), cvtpk(a[2], a[3]), cvtpk(a[4], a[5]), cvtpk(a[6], a[7])};
            u32x4 p1 = {cvtpk(a[8], a[9]), cvtpk(a[10], a[11]), cvtpk(a[12], a[13]), cvtpk(a[14], a[15])};
            if (i < NQ) {
                if (h) { p1[2] = (sec == 1) ? 0x00003F80u : 0u; p1[3] = 0u; }
            } else {
                if (r >= 28) { const unsigned f = (r == 28) ? 0x3F803F80u : 0u; p0 = (u32x4){f, f, f, f}; p1 = p0; }
            }
            asm volatile("global_store_dwordx4 %0, %1, off sc1\n\ts_nop 1" :: "v"(dst), "v"(p0) : "memory");
            asm volatile("global_store_dwordx4 %0, %1, off offset:1024 sc1\n\ts_nop 1" :: "v"(dst), "v"(p1) : "memory");
        }
    }
}

__global__ __launch_bounds__(512, 2) void qkv_kernel(const u16* __restrict__ xA, const u16* __restrict__ wA,
                                                     const u16* __restrict__ xB, const u16* __restrict__ wB, char* __restrict__ ws) {
    __shared__ __attribute__((aligned(16))) char lds_[Q_SLOT * Q_NSLOT];
    const LAS char* lds = (const LAS char*)lds_;
    const unsigned lds0 = (unsigned)(uintptr_t)lds_;
    const int tid = threadIdx.x, lane = tid & 63;
    const int wv = __builtin_amdgcn_readfirstlane(tid >> 6), w = wv & 3;
    const int bid = blockIdx.x, g = bid & 7, rr = bid >> 3;
    const int blk = g >> 2, tt = (g & 1) * 8 + (rr & 7), cg = ((g >> 1) & 1) * 4 + (rr >> 3);
    if (wv >= 4) {
        const u16* x = blk ? xB : xA;
        const u16* wq = blk ? wB : wA;
        const char* xs = (const char*)(x + (size_t)tt * 128 * DM);
        const char* wsrc = (const char*)(wq + cg * 168);
        unsigned xoff;
        { const int row = 8 * w + (lane >> 3), slot = lane & 7, c = slot ^ ((row >> 1) & 7); xoff = (unsigned)(row * DM * 2 + c * 16); }
        unsigned woff[6];
#pragma unroll
        for (int j = 0; j < 6; ++j) {
            int ci = (j * 4 + w) * 64 + lane; if (ci >= 1344) ci -= 1344;
            const int row = ci / 21, ch = ci - row * 21;
            woff[j] = (unsigned)(row * N3 * 2 + ch * 16);
        }
        const int koff = (rr * 3) % 7;
        auto issue = [&](int s) {
            const unsigned sl = lds0 + (unsigned)((s % Q_NSLOT) * Q_SLOT);
            int ks = s + koff; if (ks >= 7) ks -= 7;
            const char* xb = xs + ks * 128;
            const char* wb = wsrc + (size_t)ks * 64 * N3 * 2;
#pragma unroll
            for (int j = 0; j < 4; ++j) glds16(xb + (size_t)j * 32 * DM * 2, xoff, sl + (j * 4 + w) * 1024);
#pragma unroll
            for (int j = 0; j < 6; ++j) glds16(wb, woff[j], sl + Q_WOFF + (j * 4 + w) * 1024);
        };
        issue(0); issue(1);
        asm volatile("s_waitcnt vmcnt(10)\n\ts_barrier" ::: "memory");
        issue(2); issue(3);
#pragma unroll
        for (int s = 1; s < 7; ++s) {
            if (s < 4)       asm volatile("s_waitcnt vmcnt(20)\n\ts_barrier" ::: "memory");
            else if (s == 4) asm volatile("s_waitcnt vmcnt(20)\n\ts_barrier" ::: "memory");
            else if (s == 5) asm volatile("s_waitcnt vmcnt(10)\n\ts_barrier" ::: "memory");
            else             asm volatile("s_waitcnt vmcnt(0)\n\ts_barrier" ::: "memory");
            if (s + 3 < 7) issue(s + 3);
        }
        return;
    }
    const int h = lane >> 5, r = lane & 31;
    const int tp = w & 1, ht = w >> 1;
    const int xbase0 = (64 * tp + r) * 128 + ((h ^ ((r >> 1) & 7)) << 4);
    const int i16 = lane & 15, gq = (lane >> 4) & 1;
    const int wbase = Q_WOFF + (8 * h + (i16 >> 2)) * Q_WROW + (16 * gq + 4 * (i16 & 3)) * 2 + ht * 168;
    const int hs0 = cg * 6 + ht * 3;
    const int nq = hs0 >= 32 ? 0 : (32 - hs0 >= 3 ? 3 : 32 - hs0);
    const int tile0 = tt * 4 + tp * 2;
    if (nq == 3)      qkv_compute<3>(lds, xbase0, wbase, ws, blk, hs0, tile0, lane);
    else if (nq == 0) qkv_compute<0>(lds, xbase0, wbase, ws, blk, hs0, tile0, lane);
    else              qkv_compute<2>(lds, xbase0, wbase, ws, blk, hs0, tile0, lane);
}

constexpr int A_CHUNK = 16384, A_NSLOT = 4;

#define EXP1(v, i) v[i] = __builtin_amdgcn_exp2f(v[i])
DI float rowmax16(const f32x16& s) {
    float a = fmaxf(fmaxf(s[0], s[1]), s[2]);
#pragma unroll
    for (int i = 3; i < 15; i += 2) a = fmaxf(fmaxf(a, s[i]), s[i + 1]);
    a = fmaxf(a, s[15]);
    auto rr = __builtin_amdgcn_permlane32_swap(__float_as_uint(a), __float_as_uint(a), false, false);
    return fmaxf(__uint_as_float(rr[0]), __uint_as_float(rr[1]));
}
DI void pack16(const f32x16& p, bf16x8& p0, bf16x8& p1) {
    u32x4 a = {cvtpk(p[0], p[1]), cvtpk(p[2], p[3]), cvtpk(p[4], p[5]), cvtpk(p[6], p[7])};
    u32x4 b = {cvtpk(p[8], p[9]), cvtpk(p[10], p[11]), cvtpk(p[12], p[13]), cvtpk(p[14], p[15])};
    p0 = __builtin_bit_cast(bf16x8, a); p1 = __builtin_bit_cast(bf16x8, b);
}

DI void attn_slow(const char* kg, const char* vg, bf16x8 q0, bf16x8 q1, f32x16& o) {
    float m = -INFINITY;
    o = (f32x16){};
    for (int t = 0; t < 64; ++t) {
        bf16x8 k0 = *(const bf16x8*)(kg + t * 2048), k1 = *(const bf16x8*)(kg + t * 2048 + 1024);
        bf16x8 v0 = *(const bf16x8*)(vg + t * 2048), v1 = *(const bf16x8*)(vg + t * 2048 + 1024);
        f32x16 s = MFMA(k0, q0, (f32x16){}); s = MFMA(k1, q1, s);
        const float mn = fmaxf(m, rowmax16(s));
        const float alpha = (m == -INFINITY) ? 0.f : __builtin_amdgcn_exp2f(m - mn);
        m = mn;
#pragma unroll
        for (int i = 0; i < 16; ++i) { o[i] *= alpha; s[i] = __builtin_amdgcn_exp2f(s[i] - mn); }
        bf16x8 p0, p1; pack16(s, p0, p1);
        o = MFMA(v0, p0, o); o = MFMA(v1, p1, o);
    }
}

DI float bf16_round(float x) { return __uint_as_float(cvtpk(x, x) << 16); }
DI bf16x8 set_ref(bf16x8 q1, float ref, int h) {
    const short nb = (short)(cvtpk(-ref, -ref) & 0xFFFFu);
    if (h) q1[4] = nb;
    return q1;
}
__global__ __launch_bounds__(640, 3) void attn_kernel(char* __restrict__ ws, const u16* __restrict__ woA, const u16* __restrict__ woB) {
    __shared__ __attribute__((aligned(16))) char lds_[A_CHUNK * A_NSLOT];
    const LAS char* lds = (const LAS char*)lds_;
    const unsigned lds0 = (unsigned)(uintptr_t)lds_;
    const int tid = threadIdx.x, lane = tid & 63;
    const int w = __builtin_amdgcn_readfirstlane(tid >> 6);
    const int bid = blockIdx.x;
    const int bh = (bid & 7) * 4 + ((bid >> 3) & 3), qb = bid >> 5;
    const char* kbase = ws + WS_K + (size_t)bh * 131072;
    const char* vbase = ws + WS_V + (size_t)bh * 131072;
    if (w >= 8) {
        const int hf = w - 8;
        const unsigned goff = (unsigned)(lane * 16);
        auto issue = [&](int c) {
            const unsigned sl = lds0 + (unsigned)((c & (A_NSLOT - 1)) * A_CHUNK) + hf * 8192;
#pragma unroll
            for (int j = 0; j < 4; ++j) {
                glds16(kbase + hf * 65536 + c * 4096 + j * 1024, goff, sl + j * 1024);
                glds16(vbase + hf * 65536 + c * 4096 + j * 1024, goff, sl + 4096 + j * 1024);
            }
        };
        issue(1); issue(2);
        unsigned pf;
        {
            const char* wp = (const char*)(hf ? woB : woA) + (size_t)bid * 1568 + (lane < 13 ? lane * 128 : 0);
            asm volatile("global_load_dword %0, %1, off" : "=v"(pf) : "v"(wp) : "memory");
        }
        asm volatile("s_barrier" ::: "memory");
        for (int c = 0; c < 16; ++c) {
            if (c < 2)       asm volatile("s_waitcnt vmcnt(9)\n\ts_barrier" ::: "memory");
            else if (c < 14) asm volatile("s_waitcnt vmcnt(8)\n\ts_barrier" ::: "memory");
            else             asm volatile("s_waitcnt vmcnt(0)\n\ts_barrier" ::: "memory");
            if (c + 3 < 16) issue(c + 3);
        }
        asm volatile("" :: "v"(pf));
        asm volatile("s_barrier" ::: "memory");
        asm volatile("s_barrier" ::: "memory");
        return;
    }
    const int rg = w & 3, kh = w >> 2;
    const int h = lane >> 5, r = lane & 31;
    const char* qp = ws + WS_Q + (size_t)bh * 131072 + (size_t)(qb * 8 + rg * 2) * 2048 + lane * 16;
    glds16(kbase + kh * 65536 + rg * 1024, (unsigned)(lane * 16), lds0 + kh * 8192 + rg * 1024);
    glds16(vbase + kh * 65536 + rg * 1024, (unsigned)(lane * 16), lds0 + kh * 8192 + 4096 + rg * 1024);
    bf16x8 qa0 = *(const bf16x8*)qp, qa1 = *(const bf16x8*)(qp + 1024), qb0 = *(const bf16x8*)(qp + 2048), qb1 = *(const bf16x8*)(qp + 3072);

    f32x16 oa = (f32x16){}, ob = (f32x16){};
    f32x16 sa, sb;
    const LAS char* lp = lds + kh * 8192 + lane * 16;
    asm volatile("s_waitcnt vmcnt(0) lgkmcnt(0)\n\ts_barrier" ::: "memory");
    bf16x8 kn0, kn1, vc0, vc1;
    float ma, mb;
    {
        bf16x8 k0 = ldsread16(lp), k1 = ldsread16(lp + 1024);
        kn0 = ldsread16(lp + 2048); kn1 = ldsread16(lp + 3072);
        vc0 = ldsread16(lp + 4096); vc1 = ldsread16(lp + 4096 + 1024);
        sa = MFMA(k0, qa0, (f32x16){}); sa = MFMA(k1, qa1, sa);
        sb = MFMA(k0, qb0, (f32x16){}); sb = MFMA(k1, qb1, sb);
        ma = bf16_round(rowmax16(sa)); mb = bf16_round(rowmax16(sb));
#pragma unroll
        for (int i = 0; i < 16; ++i) { sa[i] -= ma; sb[i] -= mb; }
        EXP1(sa, 0);
    }
    const bf16x8 qa1r = set_ref(qa1, ma, h), qb1r = set_ref(qb1, mb, h);
    for (int c = 0; c < 16; ++c) {
        const LAS char* cb = lp + (c & (A_NSLOT - 1)) * A_CHUNK;
        const LAS char* nb = lp + ((c + 1) & (A_NSLOT - 1)) * A_CHUNK;
#pragma unroll
        for (int t = 0; t < 2; ++t) {
            if (t == 0) asm volatile("s_waitcnt lgkmcnt(0)\n\ts_barrier" ::: "memory");
            const LAS char* kp = nb + t * 2048;
            const LAS char* vp = (t == 0) ? cb + 4096 + 2048 : nb + 4096;
            bf16x8 kx0 = ldsread16(kp), kx1 = ldsread16(kp + 1024);
            EXP1(sa, 1); EXP1(sa, 2); EXP1(sa, 3); EXP1(sa, 4); SB();
            f32x16 sna = MFMA(kn0, qa0, (f32x16){}); PIN(sna); SB();
            EXP1(sa, 5); EXP1(sa, 6); EXP1(sa, 7);
            u32x4 pp = {cvtpk(sa[0], sa[1]), cvtpk(sa[2], sa[3]), cvtpk(sa[4], sa[5]), cvtpk(sa[6], sa[7])}; PIN(pp); SB();
            EXP1(sa, 8); PIN(sa); SB();
            oa = MFMA(vc0, __builtin_bit_cast(bf16x8, pp), oa); PIN(oa); SB();
            bf16x8 vn0 = ldsread16(vp), vn1 = ldsread16(vp + 1024);
            EXP1(sa, 9); EXP1(sa, 10); EXP1(sa, 11); EXP1(sa, 12); SB();
            sna = MFMA(kn1, qa1r, sna); PIN(sna); SB();
            EXP1(sa, 13); EXP1(sa, 14); EXP1(sa, 15);
            u32x4 pq = {cvtpk(sa[8], sa[9]), cvtpk(sa[10], sa[11]), cvtpk(sa[12], sa[13]), cvtpk(sa[14], sa[15])}; PIN(pq); SB();
            EXP1(sb, 0); PIN(sb); SB();
            oa = MFMA(vc1, __builtin_bit_cast(bf16x8, pq), oa); PIN(oa); SB();
            EXP1(sb, 1); EXP1(sb, 2); EXP1(sb, 3); EXP1(sb, 4); SB();
            f32x16 snb = MFMA(kn0, qb0, (f32x16){}); PIN(snb); SB();
            EXP1(sb, 5); EXP1(sb, 6); EXP1(sb, 7);
            u32x4 pr = {cvtpk(sb[0], sb[1]), cvtpk(sb[2], sb[3]), cvtpk(sb[4], sb[5]), cvtpk(sb[6], sb[7])}; PIN(pr); SB();
            EXP1(sb, 8); PIN(sb); SB();
            ob = MFMA(vc0, __builtin_bit_cast(bf16x8, pr), ob); PIN(ob); SB();
            EXP1(sb, 9); EXP1(sb, 10); EXP1(sb, 11); EXP1(sb, 12); SB();
            snb = MFMA(kn1, qb1r, snb); PIN(snb); SB();
            EXP1(sb, 13); EXP1(sb, 14); EXP1(sb, 15);
            u32x4 ps = {cvtpk(sb[8], sb[9]), cvtpk(sb[10], sb[11]), cvtpk(sb[12], sb[13]), cvtpk(sb[14], sb[15])}; PIN(ps); SB();
            EXP1(sna, 0); PIN(sna); SB();
            ob = MFMA(vc1, __builtin_bit_cast(bf16x8, ps), ob); PIN(ob); SB();
            sa = sna; sb = snb; vc0 = vn0; vc1 = vn1; kn0 = kx0; kn1 = kx1;
        }
    }
    asm volatile("s_waitcnt lgkmcnt(0)\n\ts_barrier" ::: "memory");
    {
        LAS float* xw = (LAS float*)lds_ + w * (17 * 64) + lane;
        const f32x16& give = kh ? oa : ob;
#pragma unroll
        for (int i = 0; i < 16; ++i) xw[i * 64] = give[i];
        xw[16 * 64] = kh ? ma : mb;
    }
    asm volatile("s_waitcnt lgkmcnt(0)\n\ts_barrier" ::: "memory");
    f32x16 o;
    {
        const LAS float* xr = (const LAS float*)lds_ + (w ^ 4) * (17 * 64) + lane;
        const float m1 = kh ? mb : ma, m2 = xr[16 * 64];
        const float m = fmaxf(m1, m2);
        const float f1 = __builtin_amdgcn_exp2f(m1 - m), f2 = __builtin_amdgcn_exp2f(m2 - m);
        const f32x16& mine = kh ? ob : oa;
#pragma unroll
        for (int i = 0; i < 16; ++i) o[i] = mine[i] * f1 + xr[i * 64] * f2;
    }
    {
        bool bad = false;
#pragma unroll
        for (int i = 0; i < 16; ++i) bad |= !(fabsf(o[i]) < 1.0e30f);
        if (__any(bad)) attn_slow(kbase + lane * 16, vbase + lane * 16, kh ? qb0 : qa0, kh ? qb1 : qa1, o);
    }
    const float l = __shfl(o[12], r + 32);
    const float inv = 1.0f / l;
    const int blk = bh >> 4, head = bh & 15;
    char* op = ws + WS_O + (size_t)(blk * 64 + qb * 8 + rg * 2 + kh) * 28672 + r * 16;
#pragma unroll
    for (int gg = 0; gg < 4; ++gg) {
        u32x2 v = {cvtpk(o[4 * gg] * inv, o[4 * gg + 1] * inv), cvtpk(o[4 * gg + 2] * inv, o[4 * gg + 3] * inv)};
        const int k = head * DH + 8 * gg + 4 * h;
        if (gg < 3 || h == 0) *(u32x2*)(op + (k >> 3) * 512 + (k & 7) * 2) = v;
    }
}

__global__ __launch_bounds__(256, 1) void oproj_kernel(const u16* __restrict__ woA, const u16* __restrict__ woB,
                                                       const char* __restrict__ ws, u16* __restrict__ out) {
    __shared__ __attribute__((aligned(16))) char lds_[448 * 128];
    const LAS char* lds = (const LAS char*)lds_;
    const unsigned lds0 = (unsigned)(uintptr_t)lds_;
    const int tid = threadIdx.x, lane = tid & 63;
    const int w = __builtin_amdgcn_readfirstlane(tid >> 6);
    const int bid = blockIdx.x;
    const int g = bid & 7, rr = bid >> 3;
    const int blk = g >> 2, tg = (g & 3) * 4 + (rr & 3), ng = rr >> 2;
    const u16* wo = blk ? woB : woA;
    const char* wsrc = (const char*)(wo + ng * 64);
#pragma unroll
    for (int j = 0; j < 14; ++j) {
        const int ci = (j * 4 + w) * 64 + lane, k = ci >> 3, slot = ci & 7, ch = slot ^ (((k >> 1) & 1) << 2);
        glds16(wsrc, (unsigned)(k * DM * 2 + ch * 16), lds0 + (j * 4 + w) * 1024);
    }
    const int h = lane >> 5, r = lane & 31;
    const char* ap = ws + WS_O + (size_t)(blk * 64 + tg * 4 + w) * 28672;
    const unsigned aoff = (unsigned)(lane * 16);
    bf16x8 of[28];
#pragma unroll
    for (int ks = 0; ks < 28; ++ks) asm volatile("global_load_dwordx4 %0, %1, %2" : "=v"(of[ks]) : "v"(aoff), "s"(ap + ks * 1024) : "memory");
    asm volatile("s_waitcnt vmcnt(28) lgkmcnt(0)\n\ts_barrier" ::: "memory");
    const int i16 = lane & 15, gq = (lane >> 4) & 1, q = i16 >> 2;
    const LAS char* wp0 = lds + (8 * h + q) * 128 + (((q >> 1) & 1) * 64) + (16 * gq + 4 * (i16 & 3)) * 2;
    const LAS char* wp1 = lds + (8 * h + q) * 128 + ((1 ^ ((q >> 1) & 1)) * 64) + (16 * gq + 4 * (i16 & 3)) * 2;
    f32x16 acc0 = (f32x16){}, acc1 = (f32x16){};
    bf16x8 wa[2], wb[2];
    wa[0] = cat8(trread(wp0), trread(wp0 + 512)); wb[0] = cat8(trread(wp1), trread(wp1 + 512));
#pragma unroll
    for (int ks = 0; ks < 28; ++ks) {
        const int cur = ks & 1;
        if (ks + 1 < 28) {
            wa[cur ^ 1] = cat8(trread(wp0 + (ks + 1) * 2048), trread(wp0 + (ks + 1) * 2048 + 512));
            wb[cur ^ 1] = cat8(trread(wp1 + (ks + 1) * 2048), trread(wp1 + (ks + 1) * 2048 + 512));
        }
        if ((ks & 3) == 0) {
            switch (ks >> 2) {
#define OWAIT(G, N) case G: asm volatile("s_waitcnt vmcnt(" #N ")" : "+v"(of[4 * G]), "+v"(of[4 * G + 1]), "+v"(of[4 * G + 2]), "+v"(of[4 * G + 3]) :: "memory"); break;
                OWAIT(0, 24) OWAIT(1, 20) OWAIT(2, 16) OWAIT(3, 12) OWAIT(4, 8) OWAIT(5, 4) OWAIT(6, 0)
            }
        }
        SB();
        acc0 = MFMA(wa[cur], of[ks], acc0);
        acc1 = MFMA(wb[cur], of[ks], acc1);
        SB();
    }
    const int tok = tg * 128 + w * 32 + r;
    char* dst = (char*)(out + (size_t)tok * (2 * DM) + blk * DM + ng * 64 + 8 * h);
#pragma unroll
    for (int nt = 0; nt < 2; ++nt) {
        const f32x16& acc = nt ? acc1 : acc0;
        u32x2 pk[4];
#pragma unroll
        for (int gg = 0; gg < 4; ++gg) pk[gg] = (u32x2){cvtpk(acc[4 * gg], acc[4 * gg + 1]), cvtpk(acc[4 * gg + 2], acc[4 * gg + 3])};
#pragma unroll
        for (int gg = 0; gg < 4; gg += 2) {
            auto r0 = __builtin_amdgcn_permlane32_swap(pk[gg][0], pk[gg + 1][0], false, false);
            auto r1 = __builtin_amdgcn_permlane32_swap(pk[gg][1], pk[gg + 1][1], false, false);
            u32x4 v = {r0[0], r1[0], r0[1], r1[1]};
            *(u32x4*)(dst + nt * 64 + gg * 16) = v;
        }
    }
}

extern "C" void kernel_launch(void* const* d_in, const int* in_sizes, int n_in,
                              void* d_out, int out_size, void* d_ws, size_t ws_size,
                              hipStream_t stream) {
    const u16* xA = (const u16*)d_in[0]; const u16* wA = (const u16*)d_in[1]; const u16* woA = (const u16*)d_in[2];
    const u16* xB = (const u16*)d_in[3]; const u16* wB = (const u16*)d_in[4]; const u16* woB = (const u16*)d_in[5];
    char* ws = (char*)d_ws;
    qkv_kernel<<<256, 512, 0, stream>>>(xA, wA, xB, wB, ws);
    attn_kernel<<<256, 640, 0, stream>>>(ws, woA, woB);
    oproj_kernel<<<224, 256, 0, stream>>>(woA, woB, ws, (u16*)d_out);
}
```

```cpp
#include <hip/hip_runtime.h>
#include <hip/hip_bf16.h>
#include <stdint.h>

typedef __attribute__((ext_vector_type(8))) short bf16x8;
typedef __attribute__((ext_vector_type(4))) short s16x4;
typedef __attribute__((ext_vector_type(16))) float f32x16;
typedef __attribute__((ext_vector_type(4))) unsigned u32x4;
typedef __attribute__((ext_vector_type(2))) unsigned u32x2;
typedef unsigned short u16;

#define DI __device__ __forceinline__
#define MFMA(a, b, c) __builtin_amdgcn_mfma_f32_32x32x16_bf16((a), (b), (c), 0, 0, 0)
#define LAS __attribute__((address_space(3)))
#define SB() __builtin_amdgcn_sched_barrier(0)
#define PIN(x) asm volatile("" : "+v"(x))
constexpr int SEQ = 2048, DM = 448, NH = 16, DH = 28, N3 = 1344;
constexpr size_t FRAG_BYTES = (size_t)2 * NH * 64 * 2048;
constexpr size_t WS_Q = 0, WS_K = FRAG_BYTES, WS_V = 2 * FRAG_BYTES, WS_O = 3 * FRAG_BYTES;
constexpr float QSCALE = 0.18898223650461362f * 1.4426950408889634f;

typedef __bf16 bf16x2_t __attribute__((ext_vector_type(2)));
typedef float f32x2_t __attribute__((ext_vector_type(2)));
DI unsigned cvtpk(float lo, float hi) { f32x2_t v = {lo, hi}; return __builtin_bit_cast(unsigned, __builtin_convertvector(v, bf16x2_t)); }

DI void glds16(const void* sbase, unsigned voff, unsigned lds_base) {
    unsigned sv;
    asm volatile("s_mov_b32 %0, m0\n\ts_mov_b32 m0, %3\n\ts_nop 0\n\tglobal_load_lds_dwordx4 %1, %2\n\ts_mov_b32 m0, %0"
                 : "=&s"(sv) : "v"(voff), "s"(sbase), "s"(lds_base) : "memory");
}
typedef short v4i16_t __attribute__((ext_vector_type(4)));
DI s16x4 trread(const LAS char* p) { return __builtin_bit_cast(s16x4, __builtin_amdgcn_ds_read_tr16_b64_v4i16((LAS v4i16_t*)p)); }
DI bf16x8 ldsread16(const LAS char* p) { return *(const LAS bf16x8*)p; }
DI bf16x8 cat8(s16x4 lo, s16x4 hi) { return __builtin_shufflevector(lo, hi, 0, 1, 2, 3, 4, 5, 6, 7); }

constexpr int Q_SLOT = 40960, Q_NSLOT = 4, Q_WOFF = 16384, Q_WROW = 336;

template <int NQ>
DI void qkv_compute(const LAS char* lds, int xbase0, int wbase, char* ws, int blk, int hs0, int tile0, int lane) {
    f32x16 acc[6];
#pragma unroll
    for (int i = 0; i < 6; ++i) acc[i] = (f32x16){};
    bf16x8 xa[2][2], wf[2][3];
    auto ldfr = [&](int kk, int buf) {
        const LAS char* sb = lds + ((kk >> 2) % Q_NSLOT) * Q_SLOT; const int ks = kk & 3;
        xa[buf][0] = ldsread16(sb + (xbase0 ^ (ks << 5)));
        xa[buf][1] = ldsread16(sb + ((xbase0 ^ (ks << 5)) + 4096));
#pragma unroll
        for (int i = 0; i < 3; ++i) {
            const LAS char* wp = sb + wbase + ks * 16 * Q_WROW + i * 56;
            wf[buf][i] = cat8(trread(wp), trread(wp + 4 * Q_WROW));
        }
    };
    asm volatile("s_waitcnt lgkmcnt(0)\n\ts_barrier" ::: "memory");
    ldfr(0, 0);
#pragma unroll
    for (int kk = 0; kk < 28; ++kk) {
        const int cur = kk & 1;
        if (kk + 1 < 28) {
            if (((kk + 1) & 3) == 0) { asm volatile("s_waitcnt lgkmcnt(0)\n\ts_barrier" ::: "memory"); }
            ldfr(kk + 1, cur ^ 1);
        }
        SB();
#pragma unroll
        for (int i = 0; i < 3; ++i) {
            if (i < NQ) { acc[i] = MFMA(wf[cur][i], xa[cur][0], acc[i]); acc[3 + i] = MFMA(wf[cur][i], xa[cur][1], acc[3 + i]); }
            else        { acc[i] = MFMA(xa[cur][0], wf[cur][i], acc[i]); acc[3 + i] = MFMA(xa[cur][1], wf[cur][i], acc[3 + i]); }
        }
        SB();
    }
    const int h = lane >> 5, r = lane & 31;
#pragma unroll
    for (int i = 0; i < 3; ++i) {
        const int hs = hs0 + i, sec = hs >> 4, bh = blk * 16 + (hs & 15);
        const float sc = (sec == 0) ? QSCALE : 1.0f;
#pragma unroll
        for (int st = 0; st < 2; ++st) {
            f32x16 a = acc[st * 3 + i];
            char* dst = ws + (size_t)sec * FRAG_BYTES + ((size_t)(bh * 64 + tile0 + st) * 2) * 1024 + lane * 16;
            if (i < NQ) {
#pragma unroll
                for (int j = 0; j < 16; ++j) a[j] *= sc;
            }
            u32x4 p0 = {cvtpk(a[0], a[1]), cvtpk(a[2], a[3]), cvtpk(a[4], a[5]), cvtpk(a[6], a[7])};
            u32x4 p1 = {cvtpk(a[8], a[9]), cvtpk(a[10], a[11]), cvtpk(a[12], a[13]), cvtpk(a[14], a[15])};
            if (i < NQ) {
                if (h) { p1[2] = (sec == 1) ? 0x00003F80u : 0u; p1[3] = 0u; }
            } else {
                if (r >= 28) { const unsigned f = (r == 28) ? 0x3F803F80u : 0u; p0 = (u32x4){f, f, f, f}; p1 = p0; }
            }
            asm volatile("global_store_dwordx4 %0, %1, off sc1\n\ts_nop 1" :: "v"(dst), "v"(p0) : "memory");
            asm volatile("global_store_dwordx4 %0, %1, off offset:1024 sc1\n\ts_nop 1" :: "v"(dst), "v"(p1) : "memory");
        }
    }
}

__global__ __launch_bounds__(512, 2) void qkv_kernel(const u16* __restrict__ xA, const u16* __restrict__ wA,
                                                     const u16* __restrict__ xB, const u16* __restrict__ wB, char* __restrict__ ws) {
    __shared__ __attribute__((aligned(16))) char lds_[Q_SLOT * Q_NSLOT];
    const LAS char* lds = (const LAS char*)lds_;
    const unsigned lds0 = (unsigned)(uintptr_t)lds_;
    const int tid = threadIdx.x, lane = tid & 63;
    const int wv = __builtin_amdgcn_readfirstlane(tid >> 6), w = wv & 3;
    const int bid = blockIdx.x, g = bid & 7, rr = bid >> 3;
    const int blk = g >> 2, tt = (g & 1) * 8 + (rr & 7), cg = ((g >> 1) & 1) * 4 + (rr >> 3);
    if (wv >= 4) {
        const u16* x = blk ? xB : xA;
        const u16* wq = blk ? wB : wA;
        const char* xs = (const char*)(x + (size_t)tt * 128 * DM);
        const char* wsrc = (const char*)(wq + cg * 168);
        unsigned xoff;
        { const int row = 8 * w + (lane >> 3), slot = lane & 7, c = slot ^ ((row >> 1) & 7); xoff = (unsigned)(row * DM * 2 + c * 16); }
        unsigned woff[6];
#pragma unroll
        for (int j = 0; j < 6; ++j) {
            int ci = (j * 4 + w) * 64 + lane; if (ci >= 1344) ci -= 1344;
            const int row = ci / 21, ch = ci - row * 21;
            woff[j] = (unsigned)(row * N3 * 2 + ch * 16);
        }
        const int koff = (rr * 3) % 7;
        auto issue = [&](int s) {
            const unsigned sl = lds0 + (unsigned)((s % Q_NSLOT) * Q_SLOT);
            int ks = s + koff; if (ks >= 7) ks -= 7;
            const char* xb = xs + ks * 128;
            const char* wb = wsrc + (size_t)ks * 64 * N3 * 2;
#pragma unroll
            for (int j = 0; j < 4; ++j) glds16(xb + (size_t)j * 32 * DM * 2, xoff, sl + (j * 4 + w) * 1024);
#pragma unroll
            for (int j = 0; j < 6; ++j) glds16(wb, woff[j], sl + Q_WOFF + (j * 4 + w) * 1024);
        };
        issue(0); issue(1);
        asm volatile("s_waitcnt vmcnt(10)\n\ts_barrier" ::: "memory");
        issue(2); issue(3);
#pragma unroll
        for (int s = 1; s < 7; ++s) {
            if (s < 4)       asm volatile("s_waitcnt vmcnt(20)\n\ts_barrier" ::: "memory");
            else if (s == 4) asm volatile("s_waitcnt vmcnt(20)\n\ts_barrier" ::: "memory");
            else if (s == 5) asm volatile("s_waitcnt vmcnt(10)\n\ts_barrier" ::: "memory");
            else             asm volatile("s_waitcnt vmcnt(0)\n\ts_barrier" ::: "memory");
            if (s + 3 < 7) issue(s + 3);
        }
        return;
    }
    const int h = lane >> 5, r = lane & 31;
    const int tp = w & 1, ht = w >> 1;
    const int xbase0 = (64 * tp + r) * 128 + ((h ^ ((r >> 1) & 7)) << 4);
    const int i16 = lane & 15, gq = (lane >> 4) & 1;
    const int wbase = Q_WOFF + (8 * h + (i16 >> 2)) * Q_WROW + (16 * gq + 4 * (i16 & 3)) * 2 + ht * 168;
    const int hs0 = cg * 6 + ht * 3;
    const int nq = hs0 >= 32 ? 0 : (32 - hs0 >= 3 ? 3 : 32 - hs0);
    const int tile0 = tt * 4 + tp * 2;
    if (nq == 3)      qkv_compute<3>(lds, xbase0, wbase, ws, blk, hs0, tile0, lane);
    else if (nq == 0) qkv_compute<0>(lds, xbase0, wbase, ws, blk, hs0, tile0, lane);
    else              qkv_compute<2>(lds, xbase0, wbase, ws, blk, hs0, tile0, lane);
}

constexpr int A_CHUNK = 16384, A_NSLOT = 4;

#define EXP1(v, i) v[i] = __builtin_amdgcn_exp2f(v[i])
DI float rowmax16(const f32x16& s) {
    float a = fmaxf(fmaxf(s[0], s[1]), s[2]);
#pragma unroll
    for (int i = 3; i < 15; i += 2) a = fmaxf(fmaxf(a, s[i]), s[i + 1]);
    a = fmaxf(a, s[15]);
    auto rr = __builtin_amdgcn_permlane32_swap(__float_as_uint(a), __float_as_uint(a), false, false);
    return fmaxf(__uint_as_float(rr[0]), __uint_as_float(rr[1]));
}
DI void pack16(const f32x16& p, bf16x8& p0, bf16x8& p1) {
    u32x4 a = {cvtpk(p[0], p[1]), cvtpk(p[2], p[3]), cvtpk(p[4], p[5]), cvtpk(p[6], p[7])};
    u32x4 b = {cvtpk(p[8], p[9]), cvtpk(p[10], p[11]), cvtpk(p[12], p[13]), cvtpk(p[14], p[15])};
    p0 = __builtin_bit_cast(bf16x8, a); p1 = __builtin_bit_cast(bf16x8, b);
}

DI void attn_slow(const char* kg, const char* vg, bf16x8 q0, bf16x8 q1, f32x16& o) {
    float m = -INFINITY;
    o = (f32x16){};
    for (int t = 0; t < 64; ++t) {
        bf16x8 k0 = *(const bf16x8*)(kg + t * 2048), k1 = *(const bf16x8*)(kg + t * 2048 + 1024);
        bf16x8 v0 = *(const bf16x8*)(vg + t * 2048), v1 = *(const bf16x8*)(vg + t * 2048 + 1024);
        f32x16 s = MFMA(k0, q0, (f32x16){}); s = MFMA(k1, q1, s);
        const float mn = fmaxf(m, rowmax16(s));
        const float alpha = (m == -INFINITY) ? 0.f : __builtin_amdgcn_exp2f(m - mn);
        m = mn;
#pragma unroll
        for (int i = 0; i < 16; ++i) { o[i] *= alpha; s[i] = __builtin_amdgcn_exp2f(s[i] - mn); }
        bf16x8 p0, p1; pack16(s, p0, p1);
        o = MFMA(v0, p0, o); o = MFMA(v1, p1, o);
    }
}

DI float bf16_round(float x) { return __uint_as_float(cvtpk(x, x) << 16); }
DI bf16x8 set_ref(bf16x8 q1, float ref, int h) {
    const short nb = (short)(cvtpk(-ref, -ref) & 0xFFFFu);
    if (h) q1[4] = nb;
    return q1;
}
__global__ __launch_bounds__(640, 3) void attn_kernel(char* __restrict__ ws, const u16* __restrict__ woA, const u16* __restrict__ woB) {
    __shared__ __attribute__((aligned(16))) char lds_[A_CHUNK * A_NSLOT];
    const LAS char* lds = (const LAS char*)lds_;
    const unsigned lds0 = (unsigned)(uintptr_t)lds_;
    const int tid = threadIdx.x, lane = tid & 63;
    const int w = __builtin_amdgcn_readfirstlane(tid >> 6);
    const int bid = blockIdx.x;
    const int bh = (bid & 7) * 4 + ((bid >> 3) & 3), qb = bid >> 5;
    const char* kbase = ws + WS_K + (size_t)bh * 131072;
    const char* vbase = ws + WS_V + (size_t)bh * 131072;
    if (w >= 8) {
        const int hf = w - 8;
        const unsigned goff = (unsigned)(lane * 16);
        auto issue = [&](int c) {
            const unsigned sl = lds0 + (unsigned)((c & (A_NSLOT - 1)) * A_CHUNK) + hf * 8192;
#pragma unroll
            for (int j = 0; j < 4; ++j) {
                glds16(kbase + hf * 65536 + c * 4096 + j * 1024, goff, sl + j * 1024);
                glds16(vbase + hf * 65536 + c * 4096 + j * 1024, goff, sl + 4096 + j * 1024);
            }
        };
        issue(2);
        unsigned pf;
        {
            const char* wp = (const char*)(hf ? woB : woA) + (size_t)bid * 1568 + (lane < 13 ? lane * 128 : 0);
            asm volatile("global_load_dword %0, %1, off" : "=v"(pf) : "v"(wp) : "memory");
        }
        asm volatile("s_barrier" ::: "memory");
        for (int c = 0; c < 16; ++c) {
            if (c == 0)      asm volatile("s_barrier" ::: "memory");
            else if (c == 1) asm volatile("s_waitcnt vmcnt(9)\n\ts_barrier" ::: "memory");
            else if (c < 14) asm volatile("s_waitcnt vmcnt(8)\n\ts_barrier" ::: "memory");
            else             asm volatile("s_waitcnt vmcnt(0)\n\ts_barrier" ::: "memory");
            if (c + 3 < 16) issue(c + 3);
        }
        asm volatile("" :: "v"(pf));
        asm volatile("s_barrier" ::: "memory");
        asm volatile("s_barrier" ::: "memory");
        return;
    }
    const int rg = w & 3, kh = w >> 2;
    const int h = lane >> 5, r = lane & 31;
    const char* qp = ws + WS_Q + (size_t)bh * 131072 + (size_t)(qb * 8 + rg * 2) * 2048 + lane * 16;
#pragma unroll
    for (int c = 0; c < 2; ++c) {
        glds16(kbase + kh * 65536 + c * 4096 + rg * 1024, (unsigned)(lane * 16), lds0 + c * A_CHUNK + kh * 8192 + rg * 1024);
        glds16(vbase + kh * 65536 + c * 4096 + rg * 1024, (unsigned)(lane * 16), lds0 + c * A_CHUNK + kh * 8192 + 4096 + rg * 1024);
    }
    bf16x8 qa0 = *(const bf16x8*)qp, qa1 = *(const bf16x8*)(qp + 1024), qb0 = *(const bf16x8*)(qp + 2048), qb1 = *(const bf16x8*)(qp + 3072);

    f32x16 oa = (f32x16){}, ob = (f32x16){};
    f32x16 sa, sb;
    const LAS char* lp = lds + kh * 8192 + lane * 16;
    asm volatile("s_waitcnt vmcnt(0) lgkmcnt(0)\n\ts_barrier" ::: "memory");
    bf16x8 kn0, kn1, vc0, vc1;
    float ma, mb;
    {
        bf16x8 k0 = ldsread16(lp), k1 = ldsread16(lp + 1024);
        kn0 = ldsread16(lp + 2048); kn1 = ldsread16(lp + 3072);
        vc0 = ldsread16(lp + 4096); vc1 = ldsread16(lp + 4096 + 1024);
        sa = MFMA(k0, qa0, (f32x16){}); sa = MFMA(k1, qa1, sa);
        sb = MFMA(k0, qb0, (f32x16){}); sb = MFMA(k1, qb1, sb);
        ma = bf16_round(rowmax16(sa)); mb = bf16_round(rowmax16(sb));
#pragma unroll
        for (int i = 0; i < 16; ++i) { sa[i] -= ma; sb[i] -= mb; }
        EXP1(sa, 0);
    }
    const bf16x8 qa1r = set_ref(qa1, ma, h), qb1r = set_ref(qb1, mb, h);
    for (int c = 0; c < 16; ++c) {
        const LAS char* cb = lp + (c & (A_NSLOT - 1)) * A_CHUNK;
        const LAS char* nb = lp + ((c + 1) & (A_NSLOT - 1)) * A_CHUNK;
#pragma unroll
        for (int t = 0; t < 2; ++t) {
            if (t == 0) asm volatile("s_waitcnt lgkmcnt(0)\n\ts_barrier" ::: "memory");
            const LAS char* kp = nb + t * 2048;
            const LAS char* vp = (t == 0) ? cb + 4096 + 2048 : nb + 4096;
            bf16x8 kx0 = ldsread16(kp), kx1 = ldsread16(kp + 1024);
            EXP1(sa, 1); EXP1(sa, 2); EXP1(sa, 3); EXP1(sa, 4); SB();
            f32x16 sna = MFMA(kn0, qa0, (f32x16){}); PIN(sna); SB();
            EXP1(sa, 5); EXP1(sa, 6); EXP1(sa, 7);
            u32x4 pp = {cvtpk(sa[0], sa[1]), cvtpk(sa[2], sa[3]), cvtpk(sa[4], sa[5]), cvtpk(sa[6], sa[7])}; PIN(pp); SB();
            EXP1(sa, 8); PIN(sa); SB();
            oa = MFMA(vc0, __builtin_bit_cast(bf16x8, pp), oa); PIN(oa); SB();
            bf16x8 vn0 = ldsread16(vp), vn1 = ldsread16(vp + 1024);
            EXP1(sa, 9); EXP1(sa, 10); EXP1(sa, 11); EXP1(sa, 12); SB();
            sna = MFMA(kn1, qa1r, sna); PIN(sna); SB();
            EXP1(sa, 13); EXP1(sa, 14); EXP1(sa, 15);
            u32x4 pq = {cvtpk(sa[8], sa[9]), cvtpk(sa[10], sa[11]), cvtpk(sa[12], sa[13]), cvtpk(sa[14], sa[15])}; PIN(pq); SB();
            EXP1(sb, 0); PIN(sb); SB();
            oa = MFMA(vc1, __builtin_bit_cast(bf16x8, pq), oa); PIN(oa); SB();
            EXP1(sb, 1); EXP1(sb, 2); EXP1(sb, 3); EXP1(sb, 4); SB();
            f32x16 snb = MFMA(kn0, qb0, (f32x16){}); PIN(snb); SB();
            EXP1(sb, 5); EXP1(sb, 6); EXP1(sb, 7);
            u32x4 pr = {cvtpk(sb[0], sb[1]), cvtpk(sb[2], sb[3]), cvtpk(sb[4], sb[5]), cvtpk(sb[6], sb[7])}; PIN(pr); SB();
            EXP1(sb, 8); PIN(sb); SB();
            ob = MFMA(vc0, __builtin_bit_cast(bf16x8, pr), ob); PIN(ob); SB();
            EXP1(sb, 9); EXP1(sb, 10); EXP1(sb, 11); EXP1(sb, 12); SB();
            snb = MFMA(kn1, qb1r, snb); PIN(snb); SB();
            EXP1(sb, 13); EXP1(sb, 14); EXP1(sb, 15);
            u32x4 ps = {cvtpk(sb[8], sb[9]), cvtpk(sb[10], sb[11]), cvtpk(sb[12], sb[13]), cvtpk(sb[14], sb[15])}; PIN(ps); SB();
            EXP1(sna, 0); PIN(sna); SB();
            ob = MFMA(vc1, __builtin_bit_cast(bf16x8, ps), ob); PIN(ob); SB();
            sa = sna; sb = snb; vc0 = vn0; vc1 = vn1; kn0 = kx0; kn1 = kx1;
        }
    }
    asm volatile("s_waitcnt lgkmcnt(0)\n\ts_barrier" ::: "memory");
    {
        LAS float* xw = (LAS float*)lds_ + w * (17 * 64) + lane;
        const f32x16& give = kh ? oa : ob;
#pragma unroll
        for (int i = 0; i < 16; ++i) xw[i * 64] = give[i];
        xw[16 * 64] = kh ? ma : mb;
    }
    asm volatile("s_waitcnt lgkmcnt(0)\n\ts_barrier" ::: "memory");
    f32x16 o;
    {
        const LAS float* xr = (const LAS float*)lds_ + (w ^ 4) * (17 * 64) + lane;
        const float m1 = kh ? mb : ma, m2 = xr[16 * 64];
        const float m = fmaxf(m1, m2);
        const float f1 = __builtin_amdgcn_exp2f(m1 - m), f2 = __builtin_amdgcn_exp2f(m2 - m);
        const f32x16& mine = kh ? ob : oa;
#pragma unroll
        for (int i = 0; i < 16; ++i) o[i] = mine[i] * f1 + xr[i * 64] * f2;
    }
    {
        bool bad = false;
#pragma unroll
        for (int i = 0; i < 16; ++i) bad |= !(fabsf(o[i]) < 1.0e30f);
        if (__any(bad)) attn_slow(kbase + lane * 16, vbase + lane * 16, kh ? qb0 : qa0, kh ? qb1 : qa1, o);
    }
    const float l = __shfl(o[12], r + 32);
    const float inv = 1.0f / l;
    const int blk = bh >> 4, head = bh & 15;
    char* op = ws + WS_O + (size_t)(blk * 64 + qb * 8 + rg * 2 + kh) * 28672 + r * 16;
#pragma unroll
    for (int gg = 0; gg < 4; ++gg) {
        u32x2 v = {cvtpk(o[4 * gg] * inv, o[4 * gg + 1] * inv), cvtpk(o[4 * gg + 2] * inv, o[4 * gg + 3] * inv)};
        const int k = head * DH + 8 * gg + 4 * h;
        if (gg < 3 || h == 0) *(u32x2*)(op + (k >> 3) * 512 + (k & 7) * 2) = v;
    }
}

__global__ __launch_bounds__(256, 1) void oproj_kernel(const u16* __restrict__ woA, const u16* __restrict__ woB,
                                                       const char* __restrict__ ws, u16* __restrict__ out) {
    __shared__ __attribute__((aligned(16))) char lds_[448 * 128];
    const LAS char* lds = (const LAS char*)lds_;
    const unsigned lds0 = (unsigned)(uintptr_t)lds_;
    const int tid = threadIdx.x, lane = tid & 63;
    const int w = __builtin_amdgcn_readfirstlane(tid >> 6);
    const int bid = blockIdx.x;
    const int g = bid & 7, rr = bid >> 3;
    const int blk = g >> 2, tg = (g & 3) * 4 + (rr & 3), ng = rr >> 2;
    const u16* wo = blk ? woB : woA;
    const char* wsrc = (const char*)(wo + ng * 64);
#pragma unroll
    for (int j = 0; j < 14; ++j) {
        const int ci = (j * 4 + w) * 64 + lane, k = ci >> 3, slot = ci & 7, ch = slot ^ (((k >> 1) & 1) << 2);
        glds16(wsrc, (unsigned)(k * DM * 2 + ch * 16), lds0 + (j * 4 + w) * 1024);
    }
    const int h = lane >> 5, r = lane & 31;
    const char* ap = ws + WS_O + (size_t)(blk * 64 + tg * 4 + w) * 28672;
    const unsigned aoff = (unsigned)(lane * 16);
    bf16x8 of[28];
#pragma unroll
    for (int ks = 0; ks < 28; ++ks) asm volatile("global_load_dwordx4 %0, %1, %2" : "=v"(of[ks]) : "v"(aoff), "s"(ap + ks * 1024) : "memory");
    asm volatile("s_waitcnt vmcnt(28) lgkmcnt(0)\n\ts_barrier" ::: "memory");
    const int i16 = lane & 15, gq = (lane >> 4) & 1, q = i16 >> 2;
    const LAS char* wp0 = lds + (8 * h + q) * 128 + (((q >> 1) & 1) * 64) + (16 * gq + 4 * (i16 & 3)) * 2;
    const LAS char* wp1 = lds + (8 * h + q) * 128 + ((1 ^ ((q >> 1) & 1)) * 64) + (16 * gq + 4 * (i16 & 3)) * 2;
    f32x16 acc0 = (f32x16){}, acc1 = (f32x16){};
    bf16x8 wa[2], wb[2];
    wa[0] = cat8(trread(wp0), trread(wp0 + 512)); wb[0] = cat8(trread(wp1), trread(wp1 + 512));
#pragma unroll
    for (int ks = 0; ks < 28; ++ks) {
        const int cur = ks & 1;
        if (ks + 1 < 28) {
            wa[cur ^ 1] = cat8(trread(wp0 + (ks + 1) * 2048), trread(wp0 + (ks + 1) * 2048 + 512));
            wb[cur ^ 1] = cat8(trread(wp1 + (ks + 1) * 2048), trread(wp1 + (ks + 1) * 2048 + 512));
        }
        if ((ks & 3) == 0) {
            switch (ks >> 2) {
#define OWAIT(G, N) case G: asm volatile("s_waitcnt vmcnt(" #N ")" : "+v"(of[4 * G]), "+v"(of[4 * G + 1]), "+v"(of[4 * G + 2]), "+v"(of[4 * G + 3]) :: "memory"); break;
                OWAIT(0, 24) OWAIT(1, 20) OWAIT(2, 16) OWAIT(3, 12) OWAIT(4, 8) OWAIT(5, 4) OWAIT(6, 0)
            }
        }
        SB();
        acc0 = MFMA(wa[cur], of[ks], acc0);
        acc1 = MFMA(wb[cur], of[ks], acc1);
        SB();
    }
    const int tok = tg * 128 + w * 32 + r;
    char* dst = (char*)(out + (size_t)tok * (2 * DM) + blk * DM + ng * 64 + 8 * h);
#pragma unroll
    for (int nt = 0; nt < 2; ++nt) {
        const f32x16& acc = nt ? acc1 : acc0;
        u32x2 pk[4];
#pragma unroll
        for (int gg = 0; gg < 4; ++gg) pk[gg] = (u32x2){cvtpk(acc[4 * gg], acc[4 * gg + 1]), cvtpk(acc[4 * gg + 2], acc[4 * gg + 3])};
#pragma unroll
        for (int gg = 0; gg < 4; gg += 2) {
            auto r0 = __builtin_amdgcn_permlane32_swap(pk[gg][0], pk[gg + 1][0], false, false);
            auto r1 = __builtin_amdgcn_permlane32_swap(pk[gg][1], pk[gg + 1][1], false, false);
            u32x4 v = {r0[0], r1[0], r0[1], r1[1]};
            *(u32x4*)(dst + nt * 64 + gg * 16) = v;
        }
    }
}

extern "C" void kernel_launch(void* const* d_in, const int* in_sizes, int n_in,
                              void* d_out, int out_size, void* d_ws, size_t ws_size,
                              hipStream_t stream) {
    const u16* xA = (const u16*)d_in[0]; const u16* wA = (const u16*)d_in[1]; const u16* woA = (const u16*)d_in[2];
    const u16* xB = (const u16*)d_in[3]; const u16* wB = (const u16*)d_in[4]; const u16* woB = (const u16*)d_in[5];
    char* ws = (char*)d_ws;
    qkv_kernel<<<256, 512, 0, stream>>>(xA, wA, xB, wB, ws);
    attn_kernel<<<256, 640, 0, stream>>>(ws, woA, woB);
    oproj_kernel<<<224, 256, 0, stream>>>(woA, woB, ws, (u16*)d_out);
}
```
